```python
import torch

def _softmax(x, axis=-1):
    return torch.softmax(x, dim=axis)
def _gelu(x):
    return torch.nn.functional.gelu(x, approximate="tanh")
def _silu(x):
    return torch.nn.functional.silu(x)
def _sigmoid(x):
    return torch.sigmoid(x)
def _layernorm(x, axis=-1, eps=1e-5):
    m = x.mean(dim=axis, keepdim=True)
    v = x.var(dim=axis, keepdim=True, unbiased=False)
    return (x - m) / torch.sqrt(v + eps)
def _scatter_add(tgt, idx, src, axis=0):
    return tgt.clone().index_add_(axis, idx, src)
def _segment_sum(src, seg, n):
    out = torch.zeros((n,) + src.shape[1:], dtype=src.dtype)
    return out.index_add_(0, seg, src)
def _pad(x, pw):
    flat = tuple(v for pair in reversed(pw) for v in pair)
    return torch.nn.functional.pad(x, flat)

kernel_name = 'gated_mix__ssm_conv_s4__linear_attn_block__prompt'

def setup_inputs():

    _w = (lambda a: (lambda t: t.to(torch.bfloat16) if t.is_floating_point() else t)(torch.as_tensor(a)))
    def _A_setup():
        B, L, D, N = 1, 2048, 640, 192
        A_re = -(lambda _x: torch.exp(torch.as_tensor(_x)))((lambda *s: torch.randn(*s, dtype=torch.bfloat16) * 6.4)(D, N))
        A_im = torch.pi * (lambda *s: torch.randn(*s, dtype=torch.bfloat16) * 0.05)(D, N)
        B_re = (lambda *s: torch.randn(*s, dtype=torch.bfloat16) * 0.1)(D, N)
        B_im = (lambda *s: torch.randn(*s, dtype=torch.bfloat16) * 0.1)(D, N)
        C_re = (lambda *s: torch.randn(*s, dtype=torch.bfloat16) * 0.1)(D, N)
        C_im = (lambda *s: torch.randn(*s, dtype=torch.bfloat16) * 0.1)(D, N)
        log_dt = (lambda *s: torch.randn(*s, dtype=torch.bfloat16) * 0.05)(D) * 0.5 - 4.0
        Dskip = (lambda *s: torch.randn(*s, dtype=torch.bfloat16) * 0.05)(D)
        return {
            "u": _w((lambda *s: torch.randn(*s, dtype=torch.bfloat16) * 0.05)(B, L, D)),
            "A_re": _w(A_re),
            "A_im": _w(A_im),
            "B_re": _w(B_re),
            "B_im": _w(B_im),
            "C_re": _w(C_re),
            "C_im": _w(C_im),
            "log_dt": _w(log_dt),
            "Dskip": _w(Dskip),
        }
    _A_in = _A_setup()

    def _B_setup():
        B, S, D = 1, 2048, 640
        return {
            "x": _w((lambda *s: torch.randn(*s, dtype=torch.bfloat16) * 0.05)(B, S, D)),
            "wqkv": _w((lambda *s: torch.randn(*s, dtype=torch.bfloat16) * 0.4)(D, 3 * D)),
            "wo": _w((lambda *s: torch.randn(*s, dtype=torch.bfloat16) * 0.05)(D, D)),
        }
    _B_in = _B_setup()

    return {
        "A_u": _A_in["u"],
        "A_A_re": _A_in["A_re"],
        "A_A_im": _A_in["A_im"],
        "A_B_re": _A_in["B_re"],
        "A_B_im": _A_in["B_im"],
        "A_C_re": _A_in["C_re"],
        "A_C_im": _A_in["C_im"],
        "A_log_dt": _A_in["log_dt"],
        "A_Dskip": _A_in["Dskip"],
        "B_x": _B_in["x"],
        "B_wqkv": _B_in["wqkv"],
        "B_wo": _B_in["wo"],
        "gate_w": _w((lambda *s: torch.randn(*s, dtype=torch.bfloat16) * 0.05)(640) * 16.0),
    }

def reference(A_u, A_A_re, A_A_im, A_B_re, A_B_im, A_C_re, A_C_im, A_log_dt, A_Dskip, B_x, B_wqkv, B_wo, gate_w):
    def _A_ref(u, A_re, A_im, B_re, B_im, C_re, C_im, log_dt, Dskip):
        Bsz, L, D = u.shape
        dt = torch.exp(log_dt)[:, None]
        A = A_re + 1j * A_im
        Bc = B_re + 1j * B_im
        Cc = C_re + 1j * C_im
        logdA = dt * A
        dB = dt * Bc
        l = (lambda x, dt: x.to(dt))(torch.arange(L), A_re.dtype)
        P = torch.exp(logdA[:, :, None] * l[None, None, :])
        K = torch.einsum("dn,dnl,dn->dl", Cc, P, dB).real
        L2 = 2 * L
        Kp = _pad(K, ((0, 0), (0, L)))
        up = (lambda x, *d: x.permute(*d))(u, 0, 2, 1)
        up = _pad(up, ((0, 0), (0, 0), (0, L)))
        Kf = (lambda x, axes=(-2,-1): torch.fft.rfft2(x.float(), dim=axes))(Kp[:, None, :], axes=(-2, -1))
        Uf = (lambda x, axes=(-2,-1): torch.fft.rfft2(x.float(), dim=axes))(up[:, :, None, :], axes=(-2, -1))
        Yf = Uf * Kf[None, :, :, :]
        y = (lambda x, s, axes=(-2,-1): torch.fft.irfft2(x, s=s, dim=axes).to(torch.bfloat16))(Yf, (1, L2), axes=(-2, -1))[:, :, 0, :L]
        y = (lambda x, *d: x.permute(*d))(y, 0, 2, 1) + u * Dskip[None, None, :]
        return y

    def _B_ref(x, wqkv, wo):
        B, S, D = x.shape
        H = 32; Dh = D // H
        qkv = (lambda x, *d: x.permute(*d))((x @ wqkv).reshape(B, S, 3, H, Dh), 2, 0, 3, 1, 4)
        q, k, v = qkv[0], qkv[1], qkv[2]
        phi = lambda t: torch.where(t > 0, t + 1, torch.exp(t)) + 1e-6
        pq, pk = phi(q), phi(k)
        kv = torch.einsum("bhsd,bhse->bhde", pk, v)
        z = torch.einsum("bhsd->bhd", pk)
        num = torch.einsum("bhqd,bhde->bhqe", pq, kv)
        den = torch.einsum("bhqd,bhd->bhq", pq, z)[..., None] + 1e-6
        o = num / den
        return (lambda x, *d: x.permute(*d))(o, 0, 2, 1, 3).reshape(B, S, D) @ wo

    x = A_u
    g = _sigmoid((x * gate_w).sum(-1, keepdims=True))
    a = _A_ref(x, A_A_re, A_A_im, A_B_re, A_B_im, A_C_re, A_C_im, A_log_dt, A_Dskip)
    b = _B_ref(x, B_wqkv, B_wo)
    return g * a + (1 - g) * b
```

```cpp
#include <hip/hip_runtime.h>
#include <hip/hip_bf16.h>
#include <stdint.h>

typedef __bf16 bf16x8 __attribute__((ext_vector_type(8)));
typedef __bf16 bf16x2 __attribute__((ext_vector_type(2)));
typedef short s16x4 __attribute__((ext_vector_type(4)));
typedef short s16x8 __attribute__((ext_vector_type(8)));
typedef float f32x2 __attribute__((ext_vector_type(2)));
typedef float f32x4 __attribute__((ext_vector_type(4)));
typedef float f32x16 __attribute__((ext_vector_type(16)));
typedef unsigned u32x2 __attribute__((ext_vector_type(2)));
typedef unsigned u32x4 __attribute__((ext_vector_type(4)));
typedef unsigned short u16;
#define LAS __attribute__((address_space(3)))
#define DI __device__ __forceinline__

constexpr int L = 2048, D = 640, NM = 192, NH = 32, DH = 20;
constexpr size_t WS_PQF = 0;
constexpr size_t WS_PQN = WS_PQF + 2621440;
constexpr size_t WS_KVP = WS_PQN + 2621440;
constexpr size_t WS_ZP  = WS_KVP + 819200;
constexpr size_t WS_MF  = WS_ZP + 40960;
constexpr size_t WS_KR  = WS_MF + 819200;
constexpr size_t WS_UT  = WS_KR + 2662400;
constexpr size_t WS_AT  = WS_UT + 2621440;
constexpr size_t WS_G   = WS_AT + 2621440;
constexpr int KRP = 2080;

DI unsigned pk2(float lo, float hi) { f32x2 v = {lo, hi}; return __builtin_bit_cast(unsigned, __builtin_convertvector(v, bf16x2)); }
DI float bflo(unsigned u) { return __uint_as_float(u << 16); }
DI float bfhi(unsigned u) { return __uint_as_float(u & 0xffff0000u); }
DI float bf2f(u16 h) { return __uint_as_float((unsigned)h << 16); }
DI float rbf(float x) { return bflo(pk2(x, x)); }
DI bf16x8 asfrag(u32x4 v) { return __builtin_bit_cast(bf16x8, v); }
DI f32x4 mfma16(bf16x8 a, bf16x8 b, f32x4 c) { return __builtin_amdgcn_mfma_f32_16x16x32_bf16(a, b, c, 0, 0, 0); }
DI f32x16 mfma32(bf16x8 a, bf16x8 b, f32x16 c) { return __builtin_amdgcn_mfma_f32_32x32x16_bf16(a, b, c, 0, 0, 0); }
DI bf16x8 lds_frag(const char* p) { return *(const LAS bf16x8*)(const LAS char*)p; }
DI bf16x8 lds_frag_tr(const LAS char* p0, const LAS char* p1) {
    s16x4 a = __builtin_amdgcn_ds_read_tr16_b64_v4i16((LAS s16x4*)p0);
    s16x4 b = __builtin_amdgcn_ds_read_tr16_b64_v4i16((LAS s16x4*)p1);
    s16x8 ab = __builtin_shufflevector(a, b, 0, 1, 2, 3, 4, 5, 6, 7);
    return __builtin_bit_cast(bf16x8, ab);
}
#define WAIT_VM(n) asm volatile("s_waitcnt vmcnt(" #n ")" ::: "memory")
#define WAIT_LGKM0() asm volatile("s_waitcnt lgkmcnt(0)" ::: "memory")
#define BAR() __builtin_amdgcn_s_barrier()

constexpr float LOG2E = 1.4426950408889634f;
DI float phi(float q) { return __builtin_amdgcn_exp2f(fminf(q, 0.f) * LOG2E) + (fmaxf(q, 0.f) + 1e-6f); }

constexpr int K1_STAGE = 36864;
constexpr int K1_LDS = 4 * K1_STAGE;

template <bool ISQ>
DI void k1_gemm(const u16* __restrict__ x, const u16* __restrict__ wqkv, char* __restrict__ ws, LAS char* lds,
                int rb, int cgi, int wave, int lane) {
    const int g = lane >> 4, l15 = lane & 15;
    if (wave >= 4) {
        const int lw = wave - 4;
        __amdgpu_buffer_rsrc_t rx = __builtin_amdgcn_make_buffer_rsrc((void*)x, 0, L * D * 2, 0x00020000);
        __amdgpu_buffer_rsrc_t rw = __builtin_amdgcn_make_buffer_rsrc((void*)wqkv, 0, D * 3 * D * 2, 0x00020000);
        int xo[4], wo[5];
#pragma unroll
        for (int i = 0; i < 4; ++i) {
            int row = 32 * lw + 8 * i + (lane >> 3);
            int ch = (lane & 7) ^ ((row >> 1) & 7);
            xo[i] = (128 * rb + row) * (D * 2) + ch * 16;
        }
#pragma unroll
        for (int i = 0; i < 5; ++i) {
            int id = 64 * (5 * lw + i) + lane;
            int row = id / 20, c = id % 20;
            c ^= ((row >> 3) & 1) << 1;
            int col;
            if (ISQ) col = 160 * cgi + 8 * c;
            else {
                int hp = c / 10, cc = c % 10;
                col = (cc < 5 ? D + 8 * cc : 2 * D + 8 * (cc - 5)) + 80 * cgi + 40 * hp;
            }
            wo[i] = row * (3 * D * 2) + col * 2;
        }
        auto issue = [&](int s) {
            LAS char* buf = lds + (s & 3) * K1_STAGE;
#pragma unroll
            for (int i = 0; i < 4; ++i)
                __builtin_amdgcn_raw_ptr_buffer_load_lds(rx, (LAS void*)(buf + (4 * lw + i) * 1024), 16, xo[i], s * 128, 0, 0);
#pragma unroll
            for (int i = 0; i < 5; ++i)
                __builtin_amdgcn_raw_ptr_buffer_load_lds(rw, (LAS void*)(buf + 16384 + (5 * lw + i) * 1024), 16, wo[i], s * (64 * 3 * D * 2), 0, 0);
        };
        issue(0); issue(1); issue(2); issue(3);
        WAIT_VM(27); BAR();
#pragma unroll 1
        for (int s = 0; s < 6; ++s) { WAIT_VM(18); BAR(); issue(s + 4); }
        WAIT_VM(18); BAR();
        WAIT_VM(9); BAR();
        WAIT_VM(0); BAR();
        return;
    }
    const int wr = wave >> 1, wc = wave & 1;
    int xa[2];
#pragma unroll
    for (int ks = 0; ks < 2; ++ks) xa[ks] = (64 * wr + l15) * 128 + (((4 * ks + g) ^ (l15 >> 1)) * 16);
    int wb[2];
    {
        const int q = l15 >> 2, p = lane & 3, ct0 = 5 * wc;
#pragma unroll
        for (int par = 0; par < 2; ++par) {
            int adj = (g & 1) ? (((ct0 + par) & 1) ? -1 : 1) : 0;
            wb[par] = 16384 + (8 * g + q) * 320 + (2 * (ct0 + adj) + (p >> 1)) * 16 + (p & 1) * 8;
        }
    }
    f32x4 acc[4][5];
#pragma unroll
    for (int m = 0; m < 4; ++m)
#pragma unroll
        for (int c = 0; c < 5; ++c) acc[m][c] = (f32x4){0.f, 0.f, 0.f, 0.f};
    bf16x8 fx[2][4], fw[2][5];
    auto rd = [&](int b, int boff, int ks) {
        LAS char* bx = lds + boff + xa[ks];
#pragma unroll
        for (int m = 0; m < 4; ++m) fx[b][m] = *(const LAS bf16x8*)(bx + m * 2048);
#pragma unroll
        for (int c = 0; c < 5; ++c) {
            LAS char* bw = lds + boff + wb[c & 1] + c * 32 + ks * (32 * 320);
            fw[b][c] = lds_frag_tr(bw, bw + 4 * 320);
        }
    };
    auto mm = [&](int b) {
#pragma unroll
        for (int m = 0; m < 4; ++m)
#pragma unroll
            for (int c = 0; c < 5; ++c)
                acc[m][c] = ISQ ? mfma16(fw[b][c], fx[b][m], acc[m][c]) : mfma16(fx[b][m], fw[b][c], acc[m][c]);
    };
    BAR();
    rd(0, 0, 0);
#pragma unroll 1
    for (int s = 0; s < 10; ++s) {
        const int boff = (s & 3) * K1_STAGE;
        rd(1, boff, 1);
        mm(0);
        if (s < 9) {
            WAIT_LGKM0(); BAR();
            rd(0, ((s + 1) & 3) * K1_STAGE, 0);
        }
        mm(1);
    }
    if (ISQ) {
        char* pq = ws + WS_PQF;
#pragma unroll
        for (int m = 0; m < 4; ++m) {
            const int rt = 8 * rb + 4 * wr + m;
            u32x2 h[5];
#pragma unroll
            for (int c = 0; c < 5; ++c) {
                h[c][0] = pk2(phi(acc[m][c][0]), phi(acc[m][c][1]));
                h[c][1] = pk2(phi(acc[m][c][2]), phi(acc[m][c][3]));
            }
            const int s0 = 5 * cgi;
            char* base = pq + ((size_t)(rt * 20 + s0) * 64 + lane) * 16;
            if (wc == 0) {
                *(u32x4*)(base) = (u32x4){h[0][0], h[0][1], h[1][0], h[1][1]};
                *(u32x4*)(base + 1024) = (u32x4){h[2][0], h[2][1], h[3][0], h[3][1]};
                *(u32x2*)(base + 2048) = h[4];
            } else {
                *(u32x2*)(base + 2048 + 8) = h[0];
                *(u32x4*)(base + 3072) = (u32x4){h[1][0], h[1][1], h[2][0], h[2][1]};
                *(u32x4*)(base + 4096) = (u32x4){h[3][0], h[3][1], h[4][0], h[4][1]};
            }
        }
    } else {
        u32x4 F[5][2];
        const bool isk2 = l15 < 8;
#pragma unroll
        for (int mp = 0; mp < 2; ++mp)
#pragma unroll
            for (int c = 0; c < 5; ++c) {
                float v[8];
#pragma unroll
                for (int j = 0; j < 8; ++j) {
                    float a = acc[2 * mp + (j >> 2)][c][j & 3];
                    v[j] = (c < 2) ? phi(a) : (c == 2 ? (isk2 ? phi(a) : a) : a);
                }
                F[c][mp] = (u32x4){pk2(v[0], v[1]), pk2(v[2], v[3]), pk2(v[4], v[5]), pk2(v[6], v[7])};
            }
        u32x4 T2o[2];
#pragma unroll
        for (int mp = 0; mp < 2; ++mp)
#pragma unroll
            for (int j = 0; j < 4; ++j) T2o[mp][j] = isk2 ? 0x3f803f80u : F[2][mp][j];
        f32x4 Dt[8];
#pragma unroll
        for (int i = 0; i < 8; ++i) Dt[i] = (f32x4){0.f, 0.f, 0.f, 0.f};
#pragma unroll
        for (int mp = 0; mp < 2; ++mp) {
            Dt[0] = mfma16(asfrag(T2o[mp]), asfrag(F[0][mp]), Dt[0]);
            Dt[1] = mfma16(asfrag(F[3][mp]), asfrag(F[0][mp]), Dt[1]);
            Dt[2] = mfma16(asfrag(T2o[mp]), asfrag(F[1][mp]), Dt[2]);
            Dt[3] = mfma16(asfrag(F[3][mp]), asfrag(F[1][mp]), Dt[3]);
            Dt[4] = mfma16(asfrag(F[4][mp]), asfrag(F[1][mp]), Dt[4]);
            Dt[5] = mfma16(asfrag(F[3][mp]), asfrag(F[2][mp]), Dt[5]);
            Dt[6] = mfma16(asfrag(F[4][mp]), asfrag(F[2][mp]), Dt[6]);
            Dt[7] = mfma16(asfrag(T2o[mp]), asfrag(F[2][mp]), Dt[7]);
        }
        LAS char* red = lds + 2 * K1_STAGE;
        LAS f32x4* mine = (LAS f32x4*)(red + wave * 4096) + lane;
        LAS f32x4* other = (LAS f32x4*)(red + (wave ^ 2) * 4096) + lane;
        f32x4 R[4];
        if (wr == 0) {
#pragma unroll
            for (int i = 0; i < 4; ++i) { mine[i * 64] = Dt[4 + i]; R[i] = Dt[i]; }
        } else {
#pragma unroll
            for (int i = 0; i < 4; ++i) { mine[i * 64] = Dt[i]; R[i] = Dt[4 + i]; }
        }
        WAIT_LGKM0(); BAR();
#pragma unroll
        for (int i = 0; i < 4; ++i) R[i] += other[i * 64];
        const int hp = 2 * cgi + wc;
        float* kvp = (float*)(ws + WS_KVP) + ((size_t)rb * 32 + 2 * hp) * 400;
        float* zp = (float*)(ws + WS_ZP) + rb * 640 + 40 * hp;
#pragma unroll
        for (int i = 0; i < 4; ++i) {
            int at, bt;
            if (wr == 0) { at = (i & 1) ? 3 : 2; bt = i >> 1; }
            else { at = (i == 0 || i == 2) ? 4 : (i == 1 ? 3 : 2); bt = (i == 0) ? 1 : 2; }
            const int d = 16 * bt + l15;
            const int e0 = (at == 2 ? -8 : (at == 3 ? 8 : 24)) + 4 * g;
            const bool dv = d < 40;
            const int hd = d >= 20, he = e0 >= 20;
            if (dv && e0 >= 0 && hd == he) *(f32x4*)(kvp + hd * 400 + (d - 20 * hd) * 20 + (e0 - 20 * hd)) = R[i];
            if (at == 2 && g == 0 && dv) zp[d] = R[i][0];
        }
    }
}

struct cf { float x, y; };
DI cf cmul(cf a, cf b) { cf r; r.x = a.x * b.x - a.y * b.y; r.y = a.x * b.y + a.y * b.x; return r; }
DI cf cexp_(float zr, float zi_rev, float c) {
    float m = __builtin_amdgcn_exp2f(zr * c);
    float a = __builtin_amdgcn_fractf(zi_rev * c);
    cf r; r.x = m * __builtin_amdgcn_cosf(a); r.y = m * __builtin_amdgcn_sinf(a); return r;
}

constexpr int KG_ROW = 2064;
constexpr int KG_TAB = 0;
constexpr int KG_KC = 53 * KG_ROW;
constexpr int KG_UT = KG_KC + 8 * 1312;
static_assert(KG_UT + 32 * 1312 <= 163840, "lds");
constexpr int K1_LDS_ALL = (KG_UT + 32 * 1312 > K1_LDS) ? (KG_UT + 32 * 1312) : K1_LDS;

DI void kg_pass(const u16* __restrict__ Are, const u16* __restrict__ Aim, const u16* __restrict__ Bre, const u16* __restrict__ Bim,
                const u16* __restrict__ Cre, const u16* __restrict__ Cim, float dt, int ch, int pass, LAS char* lds, int wave, int lane,
                f32x16& acc) {
    const int i = ch * NM + pass * 64 + lane;
    const float zr = dt * bf2f(Are[i]), zi = dt * bf2f(Aim[i]);
    const float br = dt * bf2f(Bre[i]), bi = dt * bf2f(Bim[i]);
    const float cr = bf2f(Cre[i]), ci = bf2f(Cim[i]);
    cf w; w.x = cr * br - ci * bi; w.y = cr * bi + ci * br;
    const float zl = zr * LOG2E, zv = zi * 0.15915494309189535f;
    LAS char* tb = lds + KG_TAB + wave * 256 + lane * 4;
    auto put = [&](int row, cf v) { *(LAS unsigned*)(tb + row * KG_ROW) = pk2(v.x, v.y); };
    const cf F1 = cexp_(zl, zv, 1.f), G = cexp_(zl, zv, 256.f);
    cf E8 = cexp_(zl, zv, 8.f); E8.y = -E8.y;
    cf t = w; put(0, t);
#pragma unroll
    for (int a = 1; a < 8; ++a) { t = cmul(t, F1); put(a, t); }
    const cf F2 = cmul(F1, F1), F4 = cmul(F2, F2);
    cf wg = cmul(w, G); put(8, wg);
    t = wg;
#pragma unroll
    for (int a = 9; a < 12; ++a) { t = cmul(t, F2); put(a, t); }
    wg = cmul(wg, G); put(12, wg); put(13, cmul(wg, F4));
    wg = cmul(wg, G); put(14, wg); put(15, cmul(wg, F4));
#pragma unroll
    for (int a = 16; a < 21; ++a) { wg = cmul(wg, G); put(a, wg); }
    cf y; y.x = 1.f; y.y = 0.f; put(21, y);
    y = E8; put(22, y);
#pragma unroll
    for (int b = 2; b < 32; ++b) { y = cmul(y, E8); put(21 + b, y); }
    WAIT_LGKM0();
    const LAS char* ra = lds + KG_TAB + wave * 256 + (lane & 31) * KG_ROW + (lane >> 5) * 16;
#pragma unroll
    for (int ks = 0; ks < 8; ++ks) {
        bf16x8 a = *(const LAS bf16x8*)(ra + ks * 32);
        bf16x8 b = *(const LAS bf16x8*)(ra + 21 * KG_ROW + ks * 32);
        acc = mfma32(a, b, acc);
    }
    WAIT_LGKM0();
}

DI void kg_finish(const f32x16& acc, int ch, char* __restrict__ ws, LAS char* lds, int wave, int lane, const unsigned (&goff)[20]) {
    LAS char* kc = lds + KG_KC + wave * 1312;
    const int b = lane & 31, h = lane >> 5;
    *(LAS u32x2*)(kc + (8 * b + 4 * h) * 2) = (u32x2){pk2(acc[0], acc[1]), pk2(acc[2], acc[3])};
    *(LAS unsigned*)(kc + (h ? 384 + 2 * b : 256 + 4 * b) * 2) = pk2(acc[4], acc[5]);
    *(LAS unsigned*)(kc + (h ? 448 + 2 * b : 258 + 4 * b) * 2) = pk2(acc[6], acc[7]);
    const unsigned p0 = pk2(acc[8], acc[9]), p1 = pk2(acc[10], acc[11]);
    if (h == 0) {
        *(LAS u16*)(kc + (512 + b) * 2) = (u16)p0;
        *(LAS u16*)(kc + (544 + b) * 2) = (u16)(p0 >> 16);
        *(LAS u16*)(kc + (576 + b) * 2) = (u16)p1;
        *(LAS u16*)(kc + (608 + b) * 2) = (u16)(p1 >> 16);
    } else if (b == 0) {
        *(LAS u16*)(kc + 640 * 2) = (u16)p0;
        *(LAS u16*)(kc + 641 * 2) = 0;
    }
    WAIT_LGKM0();
    char* dst = ws + WS_KR + (size_t)ch * (KRP * 2);
#pragma unroll
    for (int i = 0; i < 5; ++i) {
        const int y = lane + 64 * i;
        if (y < KRP / 8) {
            u32x4 o;
#pragma unroll
            for (int j = 0; j < 4; ++j) {
                const unsigned go = goff[i * 4 + j];
                const unsigned lo = *(const LAS u16*)(kc + (go & 0xffffu));
                const unsigned hi = *(const LAS u16*)(kc + (go >> 16));
                o[j] = lo | (hi << 16);
            }
            *(u32x4*)(dst + y * 16) = o;
        }
    }
    WAIT_LGKM0();
}

DI unsigned kr_off(int x) {
    const int m = 2048 - x;
    if (m < 0 || m >= 2048) return 641 * 2;
    unsigned u = __float_as_uint((float)m);
    u = (u + 0x7fffu + ((u >> 16) & 1u)) & 0xffff0000u;
    const int l = (int)__uint_as_float(u);
    const int s = l < 256 ? 0 : (l < 512 ? 1 : (l < 1024 ? 2 : (l < 2048 ? 3 : 4)));
    return (unsigned)(128 * s + (l >> s)) * 2;
}

DI void k1_kgen(const u16* __restrict__ u, const u16* __restrict__ Are, const u16* __restrict__ Aim, const u16* __restrict__ Bre,
                const u16* __restrict__ Bim, const u16* __restrict__ Cre, const u16* __restrict__ Cim, const u16* __restrict__ logdt,
                const u16* __restrict__ gw, char* __restrict__ ws, LAS char* lds, int kb, int wave, int lane) {
    const int tid = wave * 64 + lane;
    {
        const int row = tid >> 4, l = tid & 15;
        const u16* src = u + (size_t)(32 * kb + row) * D;
        u32x4 v[5], gv[5];
#pragma unroll
        for (int i = 0; i < 5; ++i) { v[i] = *(const u32x4*)(src + (l + 16 * i) * 8); gv[i] = *(const u32x4*)(gw + (l + 16 * i) * 8); }
        float s = 0.f;
#pragma unroll
        for (int i = 0; i < 5; ++i) {
            *(LAS u32x4*)(lds + KG_UT + row * 1312 + (l + 16 * i) * 16) = v[i];
#pragma unroll
            for (int j = 0; j < 4; ++j) {
                s += rbf(bflo(v[i][j]) * bflo(gv[i][j]));
                s += rbf(bfhi(v[i][j]) * bfhi(gv[i][j]));
            }
        }
        s += __shfl_xor(s, 1); s += __shfl_xor(s, 2); s += __shfl_xor(s, 4); s += __shfl_xor(s, 8);
        if (l == 0) {
            const float sb = rbf(s);
            ((float*)(ws + WS_G))[32 * kb + row] = rbf(1.f / (1.f + __expf(-sb)));
        }
        __syncthreads();
        const int g = lane >> 4, q = (lane & 15) >> 2, p = lane & 3;
#pragma unroll
        for (int i = 0; i < 5; ++i) {
            const int ct = wave * 5 + i;
            const LAS char* p0 = lds + KG_UT + (8 * g + q) * 1312 + ct * 32 + p * 8;
            bf16x8 f = lds_frag_tr(p0, p0 + 4 * 1312);
            *(bf16x8*)(ws + WS_UT + ((size_t)(16 * ct + (lane & 15)) * L + 32 * kb + 8 * g) * 2) = f;
        }
    }
    unsigned goff[20];
#pragma unroll
    for (int i = 0; i < 5; ++i)
#pragma unroll
        for (int j = 0; j < 4; ++j) {
            const int x = (lane + 64 * i) * 8 + 2 * j;
            goff[i * 4 + j] = kr_off(x) | (kr_off(x + 1) << 16);
        }
    {
        const int ch = 10 * kb + wave;
        const float dt = rbf(expf(bf2f(logdt[ch])));
        f32x16 acc;
#pragma unroll
        for (int i = 0; i < 16; ++i) acc[i] = 0.f;
#pragma unroll 1
        for (int pass = 0; pass < 3; ++pass) kg_pass(Are, Aim, Bre, Bim, Cre, Cim, dt, ch, pass, lds, wave, lane, acc);
        kg_finish(acc, ch, ws, lds, wave, lane, goff);
    }
    {
        const int sub = wave / 3, pass = wave % 3;
        const int ch = 10 * kb + 8 + (sub & 1);
        f32x16 acc;
#pragma unroll
        for (int i = 0; i < 16; ++i) acc[i] = 0.f;
        if (wave < 6) {
            const float dt = rbf(expf(bf2f(logdt[ch])));
            kg_pass(Are, Aim, Bre, Bim, Cre, Cim, dt, ch, pass, lds, wave, lane, acc);
            if (pass != 0) {
                LAS char* tb = lds + KG_TAB + wave * 256 + lane * 4;
#pragma unroll
                for (int i = 0; i < 12; ++i) *(LAS float*)(tb + i * KG_ROW) = acc[i];
            }
        }
        __syncthreads();
        if (wave < 6 && pass == 0) {
#pragma unroll
            for (int o = 1; o < 3; ++o) {
                const LAS char* tb = lds + KG_TAB + (wave + o) * 256 + lane * 4;
#pragma unroll
                for (int i = 0; i < 12; ++i) acc[i] += *(const LAS float*)(tb + i * KG_ROW);
            }
            kg_finish(acc, ch, ws, lds, wave, lane, goff);
        }
    }
}

__global__ __launch_bounds__(512, 2) void k1_kernel(
    const u16* __restrict__ u, const u16* __restrict__ Are, const u16* __restrict__ Aim, const u16* __restrict__ Bre,
    const u16* __restrict__ Bim, const u16* __restrict__ Cre, const u16* __restrict__ Cim, const u16* __restrict__ logdt,
    const u16* __restrict__ wqkv, const u16* __restrict__ gw, char* __restrict__ ws) {
    extern __shared__ __attribute__((aligned(16))) char smem[];
    LAS char* lds = (LAS char*)smem;
    const int wave = __builtin_amdgcn_readfirstlane(threadIdx.x >> 6), lane = threadIdx.x & 63;
    const int xcd = blockIdx.x & 7, j = blockIdx.x >> 3;
    const int xr = xcd & 3, xc = xcd >> 2;
    if (j < 16) k1_gemm<false>(u, wqkv, ws, lds, 4 * xr + (j & 3), 4 * xc + (j >> 2), wave, lane);
    else if (j < 24) k1_gemm<true>(u, wqkv, ws, lds, 4 * xr + (j & 3), 2 * xc + ((j - 16) >> 2), wave, lane);
    else k1_kgen(u, Are, Aim, Bre, Bim, Cre, Cim, logdt, gw, ws, lds, (j - 24) * 8 + xcd, wave, lane);
}

constexpr int CV_U = 0;
constexpr int CV_R = 4608;
constexpr int CV_RED = CV_R;
constexpr int K2_LDS = CV_R + 24576;

DI void k2_conv(const u16* __restrict__ dskip, char* __restrict__ ws, LAS char* lds, int ch, int wave, int lane) {
    const int tid = wave * 64 + lane;
    const char* kr = ws + WS_KR + (size_t)ch * (KRP * 2);
    const char* ut = ws + WS_UT + (size_t)ch * (L * 2);
    {
        const u32x4 uv = *(const u32x4*)(ut + tid * 16);
        auto copies = [&](int y) {
            const u32x4 a = *(const u32x4*)(kr + y * 16);
            const u32x2 b = *(const u32x2*)(kr + y * 16 + 16);
            const unsigned s0 = __builtin_amdgcn_alignbyte(a[1], a[0], 2), s1 = __builtin_amdgcn_alignbyte(a[2], a[1], 2),
                           s2 = __builtin_amdgcn_alignbyte(a[3], a[2], 2), s3 = __builtin_amdgcn_alignbyte(b[0], a[3], 2),
                           s4 = __builtin_amdgcn_alignbyte(b[1], b[0], 2);
            LAS char* dst = lds + CV_R + y * 16;
            *(LAS u32x4*)(dst) = a;
            *(LAS u32x4*)(dst + 4160) = (u32x4){s0, s1, s2, s3};
            *(LAS u32x4*)(dst + 2 * 4160) = (u32x4){a[1], a[2], a[3], b[0]};
            *(LAS u32x4*)(dst + 3 * 4160) = (u32x4){s1, s2, s3, s4};
        };
        copies(tid);
        if (tid < 2) copies(256 + tid);
        *(LAS u32x4*)(lds + CV_U + 512 + tid * 16) = uv;
        if (tid < 32) *(LAS u32x4*)(lds + CV_U + tid * 16) = (u32x4){0, 0, 0, 0};
    }
    __syncthreads();
    const int r = lane & 15, g = lane >> 4;
    f32x4 acc[8];
#pragma unroll
    for (int c = 0; c < 8; ++c) acc[c] = (f32x4){0.f, 0.f, 0.f, 0.f};
    {
        const int p = (-r) & 3;
        const int abase = CV_R + p * 4160 + 2 * (2032 - r - p + 8 * g) - 512 * 7;
        const int bbase = CV_U + 2 * (240 + 16 * r + 8 * g);
#pragma unroll 1
        for (int rr = 0; rr < 2; ++rr) {
            const int rho = 2 * wave + rr;
            const LAS char* pa = lds + abase - 64 * rho;
            const LAS char* pb = lds + bbase - 64 * rho;
            bf16x8 fb[8];
#pragma unroll
            for (int b = 0; b < 8; ++b) fb[b] = *(const LAS bf16x8*)(pb + 512 * b);
#pragma unroll
            for (int a = 0; a < 8; ++a) {
                const u32x2 lo = *(const LAS u32x2*)(pa + 512 * (7 - a));
                const u32x2 hi = *(const LAS u32x2*)(pa + 512 * (7 - a) + 8);
                const bf16x8 fa = asfrag((u32x4){lo[0], lo[1], hi[0], hi[1]});
#pragma unroll
                for (int b = 0; b + a < 8; ++b) acc[a + b] = mfma16(fa, fb[b], acc[a + b]);
            }
        }
    }
    __syncthreads();
    f32x4 o0, o1;
#pragma unroll
    for (int c = 0; c < 8; ++c) {
        if ((c >> 1) != wave) {
            const int k = c - (c > 2 * wave + 1 ? 2 : 0);
            *(LAS f32x4*)(lds + CV_RED + (wave * 6 + k) * 1024 + lane * 16) = acc[c];
        } else if (c & 1) o1 = acc[c];
        else o0 = acc[c];
    }
    __syncthreads();
#pragma unroll
    for (int s = 0; s < 4; ++s) {
        if (s != wave) {
            const int k = 2 * wave - (wave > s ? 2 : 0);
            o0 += *(const LAS f32x4*)(lds + CV_RED + (s * 6 + k) * 1024 + lane * 16);
            o1 += *(const LAS f32x4*)(lds + CV_RED + (s * 6 + k + 1) * 1024 + lane * 16);
        }
    }
    const float ds = bf2f(dskip[ch]);
#pragma unroll
    for (int i = 0; i < 2; ++i) {
        const f32x4 o = i ? o1 : o0;
        const int t = 16 * (16 * (2 * wave + i) + r) + 4 * g;
        const u32x2 uu = *(const LAS u32x2*)(lds + CV_U + 512 + t * 2);
        const float a0 = rbf(o[0]) + rbf(bflo(uu[0]) * ds), a1 = rbf(o[1]) + rbf(bfhi(uu[0]) * ds);
        const float a2 = rbf(o[2]) + rbf(bflo(uu[1]) * ds), a3 = rbf(o[3]) + rbf(bfhi(uu[1]) * ds);
        *(u32x2*)(ws + WS_AT + ((size_t)ch * L + t) * 2) = (u32x2){pk2(a0, a1), pk2(a2, a3)};
    }
}

DI void k2_mprod(const u16* __restrict__ wo, char* __restrict__ ws, LAS char* lds, int h, int cg, int wave, int lane) {
    const int tid = wave * 64 + lane;
    LAS char* kvb = lds;
    LAS char* wos = lds + 4096;
    for (int i = tid; i < 320; i += 256) {
        const int e = i / 10, c = i % 10;
        u32x4 v = (u32x4){0, 0, 0, 0};
        if (20 * h + e < D) v = *(const u32x4*)(wo + (size_t)(20 * h + e) * D + 80 * cg + 8 * c);
        *(LAS u32x4*)(wos + e * 160 + c * 16) = v;
    }
    for (int i = tid; i < 32 * 20; i += 256) ((LAS unsigned*)kvb)[i] = 0;
    __syncthreads();
    if (tid < 100) {
        const float* src = (const float*)(ws + WS_KVP) + (size_t)h * 400 + tid * 4;
        f32x4 v[16];
#pragma unroll
        for (int p = 0; p < 16; ++p) v[p] = *(const f32x4*)(src + (size_t)p * 32 * 400);
        f32x4 s = v[0];
#pragma unroll
        for (int p = 1; p < 16; ++p) s += v[p];
        const int d = tid / 5, e0 = (tid % 5) * 4;
        *(LAS u32x2*)(kvb + d * 80 + e0 * 2) = (u32x2){pk2(s[0], s[1]), pk2(s[2], s[3])};
    }
    __syncthreads();
    const int l15 = lane & 15, g = lane >> 4, q = l15 >> 2, p = lane & 3;
    for (int ct = wave; ct < 5; ct += 4) {
        const LAS char* p0 = wos + (8 * g + q) * 160 + ct * 32 + p * 8;
        const bf16x8 fb = lds_frag_tr(p0, p0 + 4 * 160);
#pragma unroll
        for (int dt = 0; dt < 2; ++dt) {
            const bf16x8 fa = *(const LAS bf16x8*)(kvb + (16 * dt + l15) * 80 + g * 16);
            f32x4 dd = mfma16(fa, fb, (f32x4){0.f, 0.f, 0.f, 0.f});
            const int gl = 4 * dt + g;
            if (gl < 5) {
                const int G = 5 * h + gl;
                const int s = G >> 3, half = (G >> 2) & 1, gg = G & 3;
                *(u32x2*)(ws + WS_MF + ((size_t)((cg * 5 + ct) * 20 + s) * 64 + l15 + 16 * gg) * 16 + half * 8) =
                    (u32x2){pk2(dd[0], dd[1]), pk2(dd[2], dd[3])};
            }
        }
    }
}

DI void k2_norm(char* __restrict__ ws, LAS char* lds, int rg, int j, int wave, int lane) {
    const int tid = wave * 64 + lane;
    const int rt = 4 * rg + wave;
    const char* src = ws + WS_PQF + ((size_t)(rt * 20 + 5 * j) * 64 + lane) * 16;
    u32x4 f[5];
#pragma unroll
    for (int s = 0; s < 5; ++s) f[s] = *(const u32x4*)(src + s * 1024);
    if (tid < 160) {
        const float* zs = (const float*)(ws + WS_ZP) + 160 * j + tid;
        float v[16];
#pragma unroll
        for (int p = 0; p < 16; ++p) v[p] = zs[p * 640];
        float s = v[0];
#pragma unroll
        for (int p = 1; p < 16; ++p) s += v[p];
        ((LAS u16*)lds)[tid] = (u16)pk2(s, s);
    }
    __syncthreads();
    const int l15 = lane & 15, g = lane >> 4;
    f32x4 den = {0.f, 0.f, 0.f, 0.f};
#pragma unroll
    for (int s = 0; s < 5; ++s) {
        const int n0 = 32 * s + 4 * g, n1 = n0 + 16;
        u32x2 z0 = *(const LAS u32x2*)(lds + n0 * 2), z1 = *(const LAS u32x2*)(lds + n1 * 2);
        if (n0 / 20 != l15) z0 = (u32x2){0, 0};
        if (n1 / 20 != l15) z1 = (u32x2){0, 0};
        den = mfma16(asfrag((u32x4){z0[0], z0[1], z1[0], z1[1]}), asfrag(f[s]), den);
    }
    LAS float* rinv = (LAS float*)(lds + 512) + wave * 16 * 12;
    if (g < 2) {
        f32x4 rv;
#pragma unroll
        for (int r = 0; r < 4; ++r) rv[r] = 1.f / rbf(rbf(den[r]) + 1e-6f);
        *(LAS f32x4*)(rinv + l15 * 12 + 4 * g) = rv;
    }
    WAIT_LGKM0();
    char* dst = ws + WS_PQN + ((size_t)(rt * 20 + 5 * j) * 64 + lane) * 16;
#pragma unroll
    for (int s = 0; s < 5; ++s) {
        const float r0 = rinv[l15 * 12 + (32 * s + 4 * g) / 20], r1 = rinv[l15 * 12 + (32 * s + 16 + 4 * g) / 20];
        u32x4 o;
        o[0] = pk2(bflo(f[s][0]) * r0, bfhi(f[s][0]) * r0);
        o[1] = pk2(bflo(f[s][1]) * r0, bfhi(f[s][1]) * r0);
        o[2] = pk2(bflo(f[s][2]) * r1, bfhi(f[s][2]) * r1);
        o[3] = pk2(bflo(f[s][3]) * r1, bfhi(f[s][3]) * r1);
        *(u32x4*)(dst + s * 1024) = o;
    }
}

__global__ __launch_bounds__(256, 4) void k2_kernel(const u16* __restrict__ dskip, const u16* __restrict__ wo, char* __restrict__ ws) {
    extern __shared__ __attribute__((aligned(16))) char smem[];
    LAS char* lds = (LAS char*)smem;
    const int wave = __builtin_amdgcn_readfirstlane(threadIdx.x >> 6), lane = threadIdx.x & 63;
    const int b = blockIdx.x;
    if (b < 128) k2_norm(ws, lds, b >> 2, b & 3, wave, lane);
    else if (b < 384) k2_mprod(wo, ws, lds, (b - 128) >> 3, (b - 128) & 7, wave, lane);
    else k2_conv(dskip, ws, lds, b - 384, wave, lane);
}

constexpr int K3_M = 0;
constexpr int K3_A = 102400;
constexpr int K3_LDS = K3_A + 10240;

__global__ __launch_bounds__(256, 1) void k3_kernel(char* __restrict__ ws, u16* __restrict__ out) {
    extern __shared__ __attribute__((aligned(16))) char smem[];
    LAS char* lds = (LAS char*)smem;
    const int wave = __builtin_amdgcn_readfirstlane(threadIdx.x >> 6), lane = threadIdx.x & 63;
    const int rb = (blockIdx.x & 7) + 8 * ((blockIdx.x >> 3) & 3), cg = blockIdx.x >> 5;
    const int l15 = lane & 15, g = lane >> 4;
    __amdgpu_buffer_rsrc_t rm = __builtin_amdgcn_make_buffer_rsrc((void*)(ws + WS_MF), 0, 819200, 0x00020000);
    __amdgpu_buffer_rsrc_t ra = __builtin_amdgcn_make_buffer_rsrc((void*)(ws + WS_AT), 0, L * D * 2, 0x00020000);
#pragma unroll
    for (int i = 0; i < 25; ++i)
        __builtin_amdgcn_raw_ptr_buffer_load_lds(rm, (LAS void*)(lds + K3_M + (wave * 25 + i) * 1024), 16, lane * 16,
                                                 cg * 102400 + (wave * 25 + i) * 1024, 0, 0);
    for (int i = wave; i < 10; i += 4)
        __builtin_amdgcn_raw_ptr_buffer_load_lds(ra, (LAS void*)(lds + K3_A + i * 1024), 16,
                                                 ((80 * cg + 8 * i + (lane >> 3)) * L + 64 * rb) * 2 + (lane & 7) * 16, 0, 0, 0);
    const int rt = 4 * rb + wave;
    const char* src = ws + WS_PQN + ((size_t)(rt * 20) * 64 + lane) * 16;
    u32x4 f[20];
#pragma unroll
    for (int s = 0; s < 20; ++s) f[s] = *(const u32x4*)(src + s * 1024);
    const float gt = ((const float*)(ws + WS_G))[16 * rt + l15];
    WAIT_VM(0);
    BAR();
    f32x4 acc[5];
#pragma unroll
    for (int c = 0; c < 5; ++c) acc[c] = (f32x4){0.f, 0.f, 0.f, 0.f};
#pragma unroll
    for (int s = 0; s < 20; ++s)
#pragma unroll
        for (int c = 0; c < 5; ++c) {
            const bf16x8 fm = *(const LAS bf16x8*)(lds + K3_M + (c * 20 + s) * 1024 + lane * 16);
            acc[c] = mfma16(fm, asfrag(f[s]), acc[c]);
        }
    const float og = rbf(1.f - gt);
    const int q = l15 >> 2, p = lane & 3;
#pragma unroll
    for (int c = 0; c < 5; ++c) {
        const s16x4 av = __builtin_amdgcn_ds_read_tr16_b64_v4i16(
            (LAS s16x4*)(lds + K3_A + (16 * c + 4 * g + q) * 128 + (16 * wave + 4 * p) * 2));
        const u32x2 aa = __builtin_bit_cast(u32x2, av);
        const float a0 = bflo(aa[0]), a1 = bfhi(aa[0]), a2 = bflo(aa[1]), a3 = bfhi(aa[1]);
        const float o0 = rbf(gt * a0) + rbf(og * rbf(acc[c][0])), o1 = rbf(gt * a1) + rbf(og * rbf(acc[c][1]));
        const float o2 = rbf(gt * a2) + rbf(og * rbf(acc[c][2])), o3 = rbf(gt * a3) + rbf(og * rbf(acc[c][3]));
        *(u32x2*)(out + (size_t)(16 * rt + l15) * D + 80 * cg + 16 * c + 4 * g) = (u32x2){pk2(o0, o1), pk2(o2, o3)};
    }
}

extern "C" void kernel_launch(void* const* d_in, const int* in_sizes, int n_in, void* d_out, int out_size, void* d_ws,
                              size_t ws_size, hipStream_t stream) {
    const u16* u = (const u16*)d_in[0];
    const u16* Are = (const u16*)d_in[1];
    const u16* Aim = (const u16*)d_in[2];
    const u16* Bre = (const u16*)d_in[3];
    const u16* Bim = (const u16*)d_in[4];
    const u16* Cre = (const u16*)d_in[5];
    const u16* Cim = (const u16*)d_in[6];
    const u16* logdt = (const u16*)d_in[7];
    const u16* dskip = (const u16*)d_in[8];
    const u16* wqkv = (const u16*)d_in[10];
    const u16* wo = (const u16*)d_in[11];
    const u16* gw = (const u16*)d_in[12];
    char* ws = (char*)d_ws;
    (void)hipFuncSetAttribute((const void*)k1_kernel, hipFuncAttributeMaxDynamicSharedMemorySize, K1_LDS_ALL);
    (void)hipFuncSetAttribute((const void*)k3_kernel, hipFuncAttributeMaxDynamicSharedMemorySize, K3_LDS);
    k1_kernel<<<256, 512, K1_LDS_ALL, stream>>>(u, Are, Aim, Bre, Bim, Cre, Cim, logdt, wqkv, gw, ws);
    k2_kernel<<<1024, 256, K2_LDS, stream>>>(dskip, wo, ws);
    k3_kernel<<<256, 256, K3_LDS, stream>>>(ws, (u16*)d_out);
}
```

```cpp
#include <hip/hip_runtime.h>
#include <hip/hip_bf16.h>
#include <stdint.h>

typedef __bf16 bf16x8 __attribute__((ext_vector_type(8)));
typedef __bf16 bf16x2 __attribute__((ext_vector_type(2)));
typedef short s16x4 __attribute__((ext_vector_type(4)));
typedef short s16x8 __attribute__((ext_vector_type(8)));
typedef float f32x2 __attribute__((ext_vector_type(2)));
typedef float f32x4 __attribute__((ext_vector_type(4)));
typedef float f32x16 __attribute__((ext_vector_type(16)));
typedef unsigned u32x2 __attribute__((ext_vector_type(2)));
typedef unsigned u32x4 __attribute__((ext_vector_type(4)));
typedef unsigned short u16;
#define LAS __attribute__((address_space(3)))
#define DI __device__ __forceinline__

constexpr int L = 2048, D = 640, NM = 192, NH = 32, DH = 20;
constexpr size_t WS_PQF = 0;
constexpr size_t WS_PQN = WS_PQF + 2621440;
constexpr size_t WS_KVP = WS_PQN + 2621440;
constexpr size_t WS_ZP  = WS_KVP + 819200;
constexpr size_t WS_MF  = WS_ZP + 40960;
constexpr size_t WS_KR  = WS_MF + 819200;
constexpr size_t WS_UT  = WS_KR + 2662400;
constexpr size_t WS_AT  = WS_UT + 2621440;
constexpr size_t WS_G   = WS_AT + 2621440;
constexpr int KRP = 2080;

DI unsigned pk2(float lo, float hi) { f32x2 v = {lo, hi}; return __builtin_bit_cast(unsigned, __builtin_convertvector(v, bf16x2)); }
DI float bflo(unsigned u) { return __uint_as_float(u << 16); }
DI float bfhi(unsigned u) { return __uint_as_float(u & 0xffff0000u); }
DI float bf2f(u16 h) { return __uint_as_float((unsigned)h << 16); }
DI float rbf(float x) { return bflo(pk2(x, x)); }
DI bf16x8 asfrag(u32x4 v) { return __builtin_bit_cast(bf16x8, v); }
DI f32x4 mfma16(bf16x8 a, bf16x8 b, f32x4 c) { return __builtin_amdgcn_mfma_f32_16x16x32_bf16(a, b, c, 0, 0, 0); }
DI f32x16 mfma32(bf16x8 a, bf16x8 b, f32x16 c) { return __builtin_amdgcn_mfma_f32_32x32x16_bf16(a, b, c, 0, 0, 0); }
DI bf16x8 lds_frag(const char* p) { return *(const LAS bf16x8*)(const LAS char*)p; }
DI bf16x8 lds_frag_tr(const LAS char* p0, const LAS char* p1) {
    s16x4 a = __builtin_amdgcn_ds_read_tr16_b64_v4i16((LAS s16x4*)p0);
    s16x4 b = __builtin_amdgcn_ds_read_tr16_b64_v4i16((LAS s16x4*)p1);
    s16x8 ab = __builtin_shufflevector(a, b, 0, 1, 2, 3, 4, 5, 6, 7);
    return __builtin_bit_cast(bf16x8, ab);
}
#define STAMP(kid, i)
#define WAIT_VM(n) asm volatile("s_waitcnt vmcnt(" #n ")" ::: "memory")
#define WAIT_LGKM0() do { __builtin_amdgcn_s_waitcnt(0xC07F); asm volatile("" ::: "memory"); } while (0)
#define BAR() __builtin_amdgcn_s_barrier()

constexpr float LOG2E = 1.4426950408889634f;
DI f32x2 phi2(f32x2 q) {
    const f32x2 a = q * LOG2E, b = q + 1.f;
    const f32x2 m = {fmaxf(__builtin_amdgcn_fmed3f(__builtin_amdgcn_exp2f(a[0]), 0.f, 1.f), b[0]),
                     fmaxf(__builtin_amdgcn_fmed3f(__builtin_amdgcn_exp2f(a[1]), 0.f, 1.f), b[1])};
    return m + 1e-6f;
}
DI unsigned pk2f(f32x2 v) { return __builtin_bit_cast(unsigned, __builtin_convertvector(v, bf16x2)); }
DI f32x2 lo2(f32x4 v) { return __builtin_shufflevector(v, v, 0, 1); }
DI f32x2 hi2(f32x4 v) { return __builtin_shufflevector(v, v, 2, 3); }

constexpr int K1_STAGE = 36864;
constexpr int K1_LDS = 3 * K1_STAGE;

template <bool ISQ>
DI void k1_gemm(const u16* __restrict__ x, const u16* __restrict__ wqkv, char* __restrict__ ws, LAS char* lds,
                int rb, int cgi, int rot, int wave, int lane) {
    const int g = lane >> 4, l15 = lane & 15;
    STAMP(0, 0);
    if (wave >= 4) {
        const int lw = wave - 4;
        __amdgpu_buffer_rsrc_t rx = __builtin_amdgcn_make_buffer_rsrc((void*)x, 0, L * D * 2, 0x00020000);
        __amdgpu_buffer_rsrc_t rw = __builtin_amdgcn_make_buffer_rsrc((void*)wqkv, 0, D * 3 * D * 2, 0x00020000);
        const int xo0 = (lane >> 3) * (D * 2) + (((lane & 7) ^ (lane >> 4)) * 16), xo1 = xo0 ^ 64;
        const int sub = (lane & 3) ^ ((lane >> 5) << 1);
        const int wo0 = (lane >> 2) * (3 * D * 2) + sub * 16;
        const int wo1 = ISQ ? wo0 : (lane >> 2) * (3 * D * 2) + (sub >> 1) * (D * 2) + (sub & 1) * 16;
        const int odd = cgi & 1;
        const int xs0 = (128 * rb + 32 * lw) * (D * 2);
        const int wsb = 16 * lw * (3 * D * 2) + (ISQ ? 320 * cgi : D * 2 + 160 * cgi);
        auto issue = [&](int t, int slot) {
            LAS char* buf = lds + slot * K1_STAGE;
            int s = t + rot; s = s >= 10 ? s - 10 : s;
#pragma unroll
            for (int i = 0; i < 4; ++i)
                __builtin_amdgcn_raw_ptr_buffer_load_lds(rx, (LAS void*)(buf + (4 * lw + i) * 1024), 16, (i & 1) ? xo1 : xo0,
                                                         xs0 + 8 * i * (D * 2) + s * 128, 0, 0);
#pragma unroll
            for (int i = 0; i < 5; ++i) {
                const int co = ISQ ? 64 * i : (i < 4 ? (i >> 1) * (D * 2) + (i & 1) * 64 + 32 * odd : (odd ? 0 : 128));
                __builtin_amdgcn_raw_ptr_buffer_load_lds(rw, (LAS void*)(buf + 16384 + i * 4096 + lw * 1024), 16, i < 4 ? wo0 : wo1,
                                                         wsb + co + s * (64 * 3 * D * 2), 0, 0);
            }
        };
        issue(0, 0); issue(1, 1);
#pragma unroll
        for (int t = 0; t < 8; ++t) { WAIT_VM(9); BAR(); issue(t + 2, (t + 2) % 3); }
        WAIT_VM(9); BAR();
        WAIT_VM(0); BAR();
        return;
    }
    const int wr = wave >> 1, wc = wave & 1;
    int xa[2];
#pragma unroll
    for (int ks = 0; ks < 2; ++ks) xa[ks] = (64 * wr + l15) * 128 + (((4 * ks + g) ^ (l15 >> 1)) * 16);
    int wb[5];
    {
        const int q = l15 >> 2, p = lane & 3;
#pragma unroll
        for (int c = 0; c < 5; ++c) {
            int pos;
            if (ISQ) pos = 2 * (5 * wc + c) + (p >> 1);
            else {
                const int i = 2 * c + (p >> 1);
                const int isv = i >= 5, kc = 5 * wc + (isv ? i - 5 : i), odd = cgi & 1;
                const int in_rest = odd ? (kc < 2) : (kc >= 8);
                pos = in_rest ? 16 + 2 * isv + (kc & 1) : 8 * isv + kc - 2 * odd;
            }
            wb[c] = 16384 + (pos >> 2) * 4096 + (8 * g + q) * 64 + ((pos & 3) ^ ((g & 1) << 1)) * 16 + (p & 1) * 8;
        }
    }
    f32x4 acc[4][5];
#pragma unroll
    for (int m = 0; m < 4; ++m)
#pragma unroll
        for (int c = 0; c < 5; ++c) acc[m][c] = (f32x4){0.f, 0.f, 0.f, 0.f};
    bf16x8 fx[2][4], fw[2][5];
    auto rd = [&](int b, int boff, int ks) {
        LAS char* bx = lds + boff + xa[ks];
        fx[b][0] = *(const LAS bf16x8*)(bx);
#pragma unroll
        for (int c = 0; c < 5; ++c) {
            LAS char* bw = lds + boff + wb[c] + ks * (32 * 64);
            fw[b][c] = lds_frag_tr(bw, bw + 4 * 64);
        }
#pragma unroll
        for (int m = 1; m < 4; ++m) fx[b][m] = *(const LAS bf16x8*)(bx + m * 2048);
    };
    auto mm = [&](int b) {
#pragma unroll
        for (int m = 0; m < 4; ++m)
#pragma unroll
            for (int c = 0; c < 5; ++c)
                acc[m][c] = ISQ ? mfma16(fw[b][c], fx[b][m], acc[m][c]) : mfma16(fx[b][m], fw[b][c], acc[m][c]);
    };
    auto interleave = [&]() {
#pragma unroll
        for (int i = 0; i < 14; ++i) {
            __builtin_amdgcn_sched_group_barrier(0x008, 1, 0);
            __builtin_amdgcn_sched_group_barrier(0x100, 1, 0);
        }
        __builtin_amdgcn_sched_group_barrier(0x008, 6, 0);
    };
    BAR();
    STAMP(0, 1);
    rd(0, 0, 0);
#pragma unroll
    for (int s = 0; s < 10; ++s) {
        const int boff = (s % 3) * K1_STAGE;
        __builtin_amdgcn_sched_barrier(0);
        rd(1, boff, 1);
        mm(0);
        interleave();
        __builtin_amdgcn_sched_barrier(0);
        if (s < 9) {
            WAIT_LGKM0(); BAR();
            __builtin_amdgcn_sched_barrier(0);
            rd(0, ((s + 1) % 3) * K1_STAGE, 0);
            mm(1);
            interleave();
        } else mm(1);
    }
    __builtin_amdgcn_sched_barrier(0);
    STAMP(0, 2);
    if (ISQ) {
        char* pq = ws + WS_PQF;
#pragma unroll
        for (int m = 0; m < 4; ++m) {
            const int rt = 8 * rb + 4 * wr + m;
            u32x2 h[5];
#pragma unroll
            for (int c = 0; c < 5; ++c) {
                h[c][0] = pk2f(phi2(lo2(acc[m][c])));
                h[c][1] = pk2f(phi2(hi2(acc[m][c])));
            }
            const int s0 = 5 * cgi;
            char* base = pq + ((size_t)(rt * 20 + s0) * 64 + lane) * 16;
            if (wc == 0) {
                *(u32x4*)(base) = (u32x4){h[0][0], h[0][1], h[1][0], h[1][1]};
                *(u32x4*)(base + 1024) = (u32x4){h[2][0], h[2][1], h[3][0], h[3][1]};
                *(u32x2*)(base + 2048) = h[4];
            } else {
                *(u32x2*)(base + 2048 + 8) = h[0];
                *(u32x4*)(base + 3072) = (u32x4){h[1][0], h[1][1], h[2][0], h[2][1]};
                *(u32x4*)(base + 4096) = (u32x4){h[3][0], h[3][1], h[4][0], h[4][1]};
            }
        }
    } else {
        u32x4 F[5][2];
        const bool isk2 = l15 < 8;
#pragma unroll
        for (int mp = 0; mp < 2; ++mp)
#pragma unroll
            for (int c = 0; c < 5; ++c) {
#pragma unroll
                for (int j = 0; j < 4; ++j) {
                    const f32x2 a = (j & 1) ? hi2(acc[2 * mp + (j >> 1)][c]) : lo2(acc[2 * mp + (j >> 1)][c]);
                    F[c][mp][j] = (c < 2) ? pk2f(phi2(a)) : (c == 2 ? (isk2 ? pk2f(phi2(a)) : pk2f(a)) : pk2f(a));
                }
            }
        u32x4 T2o[2];
#pragma unroll
        for (int mp = 0; mp < 2; ++mp)
#pragma unroll
            for (int j = 0; j < 4; ++j) T2o[mp][j] = isk2 ? 0x3f803f80u : F[2][mp][j];
        f32x4 Dt[8];
#pragma unroll
        for (int i = 0; i < 8; ++i) Dt[i] = (f32x4){0.f, 0.f, 0.f, 0.f};
#pragma unroll
        for (int mp = 0; mp < 2; ++mp) {
            Dt[0] = mfma16(asfrag(T2o[mp]), asfrag(F[0][mp]), Dt[0]);
            Dt[1] = mfma16(asfrag(F[3][mp]), asfrag(F[0][mp]), Dt[1]);
            Dt[2] = mfma16(asfrag(T2o[mp]), asfrag(F[1][mp]), Dt[2]);
            Dt[3] = mfma16(asfrag(F[3][mp]), asfrag(F[1][mp]), Dt[3]);
            Dt[4] = mfma16(asfrag(F[4][mp]), asfrag(F[1][mp]), Dt[4]);
            Dt[5] = mfma16(asfrag(F[3][mp]), asfrag(F[2][mp]), Dt[5]);
            Dt[6] = mfma16(asfrag(F[4][mp]), asfrag(F[2][mp]), Dt[6]);
            Dt[7] = mfma16(asfrag(T2o[mp]), asfrag(F[2][mp]), Dt[7]);
        }
        LAS char* red = lds + 1 * K1_STAGE;
        LAS f32x4* mine = (LAS f32x4*)(red + wave * 4096) + lane;
        LAS f32x4* other = (LAS f32x4*)(red + (wave ^ 2) * 4096) + lane;
        f32x4 R[4];
        if (wr == 0) {
#pragma unroll
            for (int i = 0; i < 4; ++i) { mine[i * 64] = Dt[4 + i]; R[i] = Dt[i]; }
        } else {
#pragma unroll
            for (int i = 0; i < 4; ++i) { mine[i * 64] = Dt[i]; R[i] = Dt[4 + i]; }
        }
        WAIT_LGKM0(); BAR();
#pragma unroll
        for (int i = 0; i < 4; ++i) R[i] += other[i * 64];
        const int hp = 2 * cgi + wc;
        float* kvp = (float*)(ws + WS_KVP) + ((size_t)rb * 32 + 2 * hp) * 400;
        float* zp = (float*)(ws + WS_ZP) + rb * 640 + 40 * hp;
#pragma unroll
        for (int i = 0; i < 4; ++i) {
            int at, bt;
            if (wr == 0) { at = (i & 1) ? 3 : 2; bt = i >> 1; }
            else { at = (i == 0 || i == 2) ? 4 : (i == 1 ? 3 : 2); bt = (i == 0) ? 1 : 2; }
            const int d = 16 * bt + l15;
            const int e0 = (at == 2 ? -8 : (at == 3 ? 8 : 24)) + 4 * g;
            const bool dv = d < 40;
            const int hd = d >= 20, he = e0 >= 20;
            if (dv && e0 >= 0 && hd == he) *(f32x4*)(kvp + hd * 400 + (d - 20 * hd) * 20 + (e0 - 20 * hd)) = R[i];
            if (at == 2 && g == 0 && dv) zp[d] = R[i][0];
        }
    }
    STAMP(0, 3);
}

typedef f32x2 cf;
DI cf rot(cf b) { return (cf){-b[1], b[0]}; }
DI cf cmul(cf a, cf b, cf br) {
    const cf t = __builtin_shufflevector(a, a, 0, 0) * b;
    return __builtin_elementwise_fma(__builtin_shufflevector(a, a, 1, 1), br, t);
}
DI cf csq(cf a) { return (cf){__builtin_fmaf(a[0], a[0], -a[1] * a[1]), (a[0] + a[0]) * a[1]}; }
DI cf cexp_(float zr, float zi_rev, float c) {
    const float m = __builtin_amdgcn_exp2f(zr * c);
    const float a = __builtin_amdgcn_fractf(zi_rev * c);
    return (cf){m * __builtin_amdgcn_cosf(a), m * __builtin_amdgcn_sinf(a)};
}
DI unsigned pk2v(cf v) { return __builtin_bit_cast(unsigned, __builtin_convertvector(v, bf16x2)); }
struct KgP { u16 are, aim, bre, bim, cre, cim; };
constexpr int KG_ROW = 2064;
constexpr int KG_TAB = 0;
constexpr int KG_KC = 53 * KG_ROW;
constexpr int KG_UT = KG_KC + 8 * 1312;
constexpr int KG_GW = KG_UT + 32 * 1280;
constexpr int K1_LDS_ALL = KG_GW + 2048;
static_assert(K1_LDS_ALL <= 163840 && K1_LDS_ALL >= K1_LDS, "lds");

DI KgP kg_load(const u16* __restrict__ Are, const u16* __restrict__ Aim, const u16* __restrict__ Bre, const u16* __restrict__ Bim,
               const u16* __restrict__ Cre, const u16* __restrict__ Cim, int ch, int pass, int lane) {
    const int i = ch * NM + pass * 64 + lane;
    KgP p; p.are = Are[i]; p.aim = Aim[i]; p.bre = Bre[i]; p.bim = Bim[i]; p.cre = Cre[i]; p.cim = Cim[i];
    return p;
}
DI void kg_pass(const KgP& P, float dt, LAS char* lds, int wave, int lane, f32x16& acc) {
    const float zr = dt * bf2f(P.are), zi = dt * bf2f(P.aim);
    const float br = dt * bf2f(P.bre), bi = dt * bf2f(P.bim);
    const float cr = bf2f(P.cre), ci = bf2f(P.cim);
    const cf w = {cr * br - ci * bi, cr * bi + ci * br};
    const float zl = zr * LOG2E, zv = zi * 0.15915494309189535f;
    const cf F1 = cexp_(zl, zv, 1.f), F128 = cexp_(zl, zv, 128.f);
    const cf F2 = csq(F1), F4 = csq(F2), F8 = csq(F4), G = csq(F128);
    const cf E8 = {F8[0], -F8[1]}, E128 = {F128[0], -F128[1]};
    cf T = w, U = cmul(w, G, rot(G)), WG = U, Y = E8, Y2 = cmul(E128, E8, rot(E8));
    asm volatile("s_mov_b32 m0, %[mx]\n\tv_cvt_pk_bf16_f32 v154, v140, v141\n\tv_cvt_pk_bf16_f32 v155, v146, v147\n\tv_mov_b32 v157, 0x3f80\n\tds_write_addtid_b32 v157 offset:43344\n\tv_pk_mul_f32 v[150:151], %[f1], v[140:141] op_sel_hi:[1,0]\n\tv_pk_mul_f32 v[152:153], %[f2], v[142:143] op_sel_hi:[1,0]\n\tds_write_addtid_b32 v154 offset:41280\n\tv_pk_fma_f32 v[140:141], v[140:141], %[f1], v[150:151] op_sel:[1,1,0] op_sel_hi:[1,0,1] neg_lo:[0,1,0]\n\tv_pk_fma_f32 v[142:143], v[142:143], %[f2], v[152:153] op_sel:[1,1,0] op_sel_hi:[1,0,1] neg_lo:[0,1,0]\n\tds_write_addtid_b32 v155 offset:45408\n\tv_cvt_pk_bf16_f32 v156, v140, v141\n\tv_cvt_pk_bf16_f32 v157, v142, v143\n\tv_pk_mul_f32 v[150:151], %[f1], v[140:141] op_sel_hi:[1,0]\n\tv_pk_mul_f32 v[152:153], %[f2], v[142:143] op_sel_hi:[1,0]\n\tds_write_addtid_b32 v156 offset:0\n\tv_pk_fma_f32 v[140:141], v[140:141], %[f1], v[150:151] op_sel:[1,1,0] op_sel_hi:[1,0,1] neg_lo:[0,1,0]\n\tv_pk_fma_f32 v[142:143], v[142:143], %[f2], v[152:153] op_sel:[1,1,0] op_sel_hi:[1,0,1] neg_lo:[0,1,0]\n\tds_write_addtid_b32 v157 offset:16512\n\tv_cvt_pk_bf16_f32 v154, v140, v141\n\tv_cvt_pk_bf16_f32 v155, v142, v143\n\tv_pk_mul_f32 v[150:151], %[f1], v[140:141] op_sel_hi:[1,0]\n\tv_pk_mul_f32 v[152:153], %[f2], v[142:143] op_sel_hi:[1,0]\n\tds_write_addtid_b32 v154 offset:2064\n\tv_pk_fma_f32 v[140:141], v[140:141], %[f1], v[150:151] op_sel:[1,1,0] op_sel_hi:[1,0,1] neg_lo:[0,1,0]\n\tv_pk_fma_f32 v[142:143], v[142:143], %[f2], v[152:153] op_sel:[1,1,0] op_sel_hi:[1,0,1] neg_lo:[0,1,0]\n\tds_write_addtid_b32 v155 offset:18576\n\tv_cvt_pk_bf16_f32 v156, v140, v141\n\tv_cvt_pk_bf16_f32 v157, v142, v143\n\tv_pk_mul_f32 v[150:151], %[f1], v[140:141] op_sel_hi:[1,0]\n\tv_pk_mul_f32 v[152:153], %[f2], v[142:143] op_sel_hi:[1,0]\n\tds_write_addtid_b32 v156 offset:4128\n\tv_pk_fma_f32 v[140:141], v[140:141], %[f1], v[150:151] op_sel:[1,1,0] op_sel_hi:[1,0,1] neg_lo:[0,1,0]\n\tv_pk_fma_f32 v[142:143], v[142:143], %[f2], v[152:153] op_sel:[1,1,0] op_sel_hi:[1,0,1] neg_lo:[0,1,0]\n\tds_write_addtid_b32 v157 offset:20640\n\tv_cvt_pk_bf16_f32 v154, v140, v141\n\tv_cvt_pk_bf16_f32 v155, v142, v143\n\tv_pk_mul_f32 v[150:151], %[f1], v[140:141] op_sel_hi:[1,0]\n\tv_pk_mul_f32 v[152:153], %[g], v[144:145] op_sel_hi:[1,0]\n\tds_write_addtid_b32 v154 offset:6192\n\tv_pk_fma_f32 v[140:141], v[140:141], %[f1], v[150:151] op_sel:[1,1,0] op_sel_hi:[1,0,1] neg_lo:[0,1,0]\n\tv_pk_fma_f32 v[144:145], v[144:145], %[g], v[152:153] op_sel:[1,1,0] op_sel_hi:[1,0,1] neg_lo:[0,1,0]\n\tds_write_addtid_b32 v155 offset:22704\n\tv_cvt_pk_bf16_f32 v156, v140, v141\n\tv_pk_mul_f32 v[150:151], %[f1], v[140:141] op_sel_hi:[1,0]\n\tv_pk_mul_f32 v[152:153], %[f4], v[144:145] op_sel_hi:[1,0]\n\tds_write_addtid_b32 v156 offset:8256\n\tv_pk_fma_f32 v[140:141], v[140:141], %[f1], v[150:151] op_sel:[1,1,0] op_sel_hi:[1,0,1] neg_lo:[0,1,0]\n\tv_pk_fma_f32 v[142:143], v[144:145], %[f4], v[152:153] op_sel:[1,1,0] op_sel_hi:[1,0,1] neg_lo:[0,1,0]\n\tv_cvt_pk_bf16_f32 v157, v140, v141\n\tv_cvt_pk_bf16_f32 v154, v142, v143\n\tv_pk_mul_f32 v[150:151], %[f1], v[140:141] op_sel_hi:[1,0]\n\tv_pk_mul_f32 v[152:153], %[f4], v[142:143] op_sel_hi:[1,0]\n\tds_write_addtid_b32 v157 offset:10320\n\tv_pk_fma_f32 v[140:141], v[140:141], %[f1], v[150:151] op_sel:[1,1,0] op_sel_hi:[1,0,1] neg_lo:[0,1,0]\n\tv_pk_fma_f32 v[142:143], v[142:143], %[f4], v[152:153] op_sel:[1,1,0] op_sel_hi:[1,0,1] neg_lo:[0,1,0]\n\tds_write_addtid_b32 v154 offset:24768\n\tv_cvt_pk_bf16_f32 v155, v140, v141\n\tv_cvt_pk_bf16_f32 v156, v142, v143\n\tv_pk_mul_f32 v[150:151], %[f1], v[140:141] op_sel_hi:[1,0]\n\tv_pk_mul_f32 v[152:153], %[g], v[144:145] op_sel_hi:[1,0]\n\tds_write_addtid_b32 v155 offset:12384\n\tv_pk_fma_f32 v[140:141], v[140:141], %[f1], v[150:151] op_sel:[1,1,0] op_sel_hi:[1,0,1] neg_lo:[0,1,0]\n\tv_pk_fma_f32 v[144:145], v[144:145], %[g], v[152:153] op_sel:[1,1,0] op_sel_hi:[1,0,1] neg_lo:[0,1,0]\n\tds_write_addtid_b32 v156 offset:26832\n\tv_cvt_pk_bf16_f32 v157, v140, v141\n\tv_pk_mul_f32 v[150:151], %[f4], v[144:145] op_sel_hi:[1,0]\n\tv_pk_mul_f32 v[152:153], %[g], v[144:145] op_sel_hi:[1,0]\n\tds_write_addtid_b32 v157 offset:14448\n\tv_pk_fma_f32 v[142:143], v[144:145], %[f4], v[150:151] op_sel:[1,1,0] op_sel_hi:[1,0,1] neg_lo:[0,1,0]\n\tv_pk_fma_f32 v[144:145], v[144:145], %[g], v[152:153] op_sel:[1,1,0] op_sel_hi:[1,0,1] neg_lo:[0,1,0]\n\tv_cvt_pk_bf16_f32 v154, v142, v143\n\tv_pk_mul_f32 v[150:151], %[f4], v[142:143] op_sel_hi:[1,0]\n\tv_pk_mul_f32 v[152:153], %[f8], v[144:145] op_sel_hi:[1,0]\n\tds_write_addtid_b32 v154 offset:28896\n\tv_pk_fma_f32 v[142:143], v[142:143], %[f4], v[150:151] op_sel:[1,1,0] op_sel_hi:[1,0,1] neg_lo:[0,1,0]\n\tv_pk_fma_f32 v[140:141], v[144:145], %[f8], v[152:153] op_sel:[1,1,0] op_sel_hi:[1,0,1] neg_lo:[0,1,0]\n\tv_cvt_pk_bf16_f32 v155, v142, v143\n\tv_cvt_pk_bf16_f32 v156, v140, v141\n\tv_pk_mul_f32 v[150:151], %[g], v[140:141] op_sel_hi:[1,0]\n\tv_pk_mul_f32 v[152:153], %[e8], v[146:147] op_sel_hi:[1,0]\n\tds_write_addtid_b32 v155 offset:30960\n\tv_pk_fma_f32 v[140:141], v[140:141], %[g], v[150:151] op_sel:[1,1,0] op_sel_hi:[1,0,1] neg_lo:[0,1,0]\n\tv_pk_fma_f32 v[146:147], v[146:147], %[e8], v[152:153] op_sel:[1,1,0] op_sel_hi:[1,0,1] neg_lo:[0,1,0]\n\tds_write_addtid_b32 v156 offset:33024\n\tv_cvt_pk_bf16_f32 v157, v140, v141\n\tv_pk_mul_f32 v[150:151], %[g], v[140:141] op_sel_hi:[1,0]\n\tds_write_addtid_b32 v157 offset:35088\n\tv_pk_fma_f32 v[140:141], v[140:141], %[g], v[150:151] op_sel:[1,1,0] op_sel_hi:[1,0,1] neg_lo:[0,1,0]\n\ts_nop 0\n\tv_cvt_pk_bf16_f32 v154, v140, v141\n\tv_pk_mul_f32 v[150:151], %[g], v[140:141] op_sel_hi:[1,0]\n\tds_write_addtid_b32 v154 offset:37152\n\tv_pk_fma_f32 v[140:141], v[140:141], %[g], v[150:151] op_sel:[1,1,0] op_sel_hi:[1,0,1] neg_lo:[0,1,0]\n\ts_nop 0\n\tv_cvt_pk_bf16_f32 v155, v140, v141\n\tds_write_addtid_b32 v155 offset:39216\n\ts_mov_b32 m0, %[my]\n\tv_cvt_pk_bf16_f32 v156, v146, v147\n\tv_cvt_pk_bf16_f32 v157, v148, v149\n\tv_pk_mul_f32 v[150:151], %[e8], v[146:147] op_sel_hi:[1,0]\n\tv_pk_mul_f32 v[152:153], %[e8], v[148:149] op_sel_hi:[1,0]\n\tds_write_addtid_b32 v156 offset:2064\n\tv_pk_fma_f32 v[146:147], v[146:147], %[e8], v[150:151] op_sel:[1,1,0] op_sel_hi:[1,0,1] neg_lo:[0,1,0]\n\tv_pk_fma_f32 v[148:149], v[148:149], %[e8], v[152:153] op_sel:[1,1,0] op_sel_hi:[1,0,1] neg_lo:[0,1,0]\n\tds_write_addtid_b32 v157 offset:33024\n\tv_cvt_pk_bf16_f32 v154, v146, v147\n\tv_cvt_pk_bf16_f32 v155, v148, v149\n\tv_pk_mul_f32 v[150:151], %[e8], v[146:147] op_sel_hi:[1,0]\n\tv_pk_mul_f32 v[152:153], %[e8], v[148:149] op_sel_hi:[1,0]\n\tds_write_addtid_b32 v154 offset:4128\n\tv_pk_fma_f32 v[146:147], v[146:147], %[e8], v[150:151] op_sel:[1,1,0] op_sel_hi:[1,0,1] neg_lo:[0,1,0]\n\tv_pk_fma_f32 v[148:149], v[148:149], %[e8], v[152:153] op_sel:[1,1,0] op_sel_hi:[1,0,1] neg_lo:[0,1,0]\n\tds_write_addtid_b32 v155 offset:35088\n\tv_cvt_pk_bf16_f32 v156, v146, v147\n\tv_cvt_pk_bf16_f32 v157, v148, v149\n\tv_pk_mul_f32 v[150:151], %[e8], v[146:147] op_sel_hi:[1,0]\n\tv_pk_mul_f32 v[152:153], %[e8], v[148:149] op_sel_hi:[1,0]\n\tds_write_addtid_b32 v156 offset:6192\n\tv_pk_fma_f32 v[146:147], v[146:147], %[e8], v[150:151] op_sel:[1,1,0] op_sel_hi:[1,0,1] neg_lo:[0,1,0]\n\tv_pk_fma_f32 v[148:149], v[148:149], %[e8], v[152:153] op_sel:[1,1,0] op_sel_hi:[1,0,1] neg_lo:[0,1,0]\n\tds_write_addtid_b32 v157 offset:37152\n\tv_cvt_pk_bf16_f32 v154, v146, v147\n\tv_cvt_pk_bf16_f32 v155, v148, v149\n\tv_pk_mul_f32 v[150:151], %[e8], v[146:147] op_sel_hi:[1,0]\n\tv_pk_mul_f32 v[152:153], %[e8], v[148:149] op_sel_hi:[1,0]\n\tds_write_addtid_b32 v154 offset:8256\n\tv_pk_fma_f32 v[146:147], v[146:147], %[e8], v[150:151] op_sel:[1,1,0] op_sel_hi:[1,0,1] neg_lo:[0,1,0]\n\tv_pk_fma_f32 v[148:149], v[148:149], %[e8], v[152:153] op_sel:[1,1,0] op_sel_hi:[1,0,1] neg_lo:[0,1,0]\n\tds_write_addtid_b32 v155 offset:39216\n\tv_cvt_pk_bf16_f32 v156, v146, v147\n\tv_cvt_pk_bf16_f32 v157, v148, v149\n\tv_pk_mul_f32 v[150:151], %[e8], v[146:147] op_sel_hi:[1,0]\n\tv_pk_mul_f32 v[152:153], %[e8], v[148:149] op_sel_hi:[1,0]\n\tds_write_addtid_b32 v156 offset:10320\n\tv_pk_fma_f32 v[146:147], v[146:147], %[e8], v[150:151] op_sel:[1,1,0] op_sel_hi:[1,0,1] neg_lo:[0,1,0]\n\tv_pk_fma_f32 v[148:149], v[148:149], %[e8], v[152:153] op_sel:[1,1,0] op_sel_hi:[1,0,1] neg_lo:[0,1,0]\n\tds_write_addtid_b32 v157 offset:41280\n\tv_cvt_pk_bf16_f32 v154, v146, v147\n\tv_cvt_pk_bf16_f32 v155, v148, v149\n\tv_pk_mul_f32 v[150:151], %[e8], v[146:147] op_sel_hi:[1,0]\n\tv_pk_mul_f32 v[152:153], %[e8], v[148:149] op_sel_hi:[1,0]\n\tds_write_addtid_b32 v154 offset:12384\n\tv_pk_fma_f32 v[146:147], v[146:147], %[e8], v[150:151] op_sel:[1,1,0] op_sel_hi:[1,0,1] neg_lo:[0,1,0]\n\tv_pk_fma_f32 v[148:149], v[148:149], %[e8], v[152:153] op_sel:[1,1,0] op_sel_hi:[1,0,1] neg_lo:[0,1,0]\n\tds_write_addtid_b32 v155 offset:43344\n\tv_cvt_pk_bf16_f32 v156, v146, v147\n\tv_cvt_pk_bf16_f32 v157, v148, v149\n\tv_pk_mul_f32 v[150:151], %[e8], v[146:147] op_sel_hi:[1,0]\n\tv_pk_mul_f32 v[152:153], %[e8], v[148:149] op_sel_hi:[1,0]\n\tds_write_addtid_b32 v156 offset:14448\n\tv_pk_fma_f32 v[146:147], v[146:147], %[e8], v[150:151] op_sel:[1,1,0] op_sel_hi:[1,0,1] neg_lo:[0,1,0]\n\tv_pk_fma_f32 v[148:149], v[148:149], %[e8], v[152:153] op_sel:[1,1,0] op_sel_hi:[1,0,1] neg_lo:[0,1,0]\n\tds_write_addtid_b32 v157 offset:45408\n\tv_cvt_pk_bf16_f32 v154, v146, v147\n\tv_cvt_pk_bf16_f32 v155, v148, v149\n\tv_pk_mul_f32 v[150:151], %[e8], v[146:147] op_sel_hi:[1,0]\n\tv_pk_mul_f32 v[152:153], %[e8], v[148:149] op_sel_hi:[1,0]\n\tds_write_addtid_b32 v154 offset:16512\n\tv_pk_fma_f32 v[146:147], v[146:147], %[e8], v[150:151] op_sel:[1,1,0] op_sel_hi:[1,0,1] neg_lo:[0,1,0]\n\tv_pk_fma_f32 v[148:149], v[148:149], %[e8], v[152:153] op_sel:[1,1,0] op_sel_hi:[1,0,1] neg_lo:[0,1,0]\n\tds_write_addtid_b32 v155 offset:47472\n\tv_cvt_pk_bf16_f32 v156, v146, v147\n\tv_cvt_pk_bf16_f32 v157, v148, v149\n\tv_pk_mul_f32 v[150:151], %[e8], v[146:147] op_sel_hi:[1,0]\n\tv_pk_mul_f32 v[152:153], %[e8], v[148:149] op_sel_hi:[1,0]\n\tds_write_addtid_b32 v156 offset:18576\n\tv_pk_fma_f32 v[146:147], v[146:147], %[e8], v[150:151] op_sel:[1,1,0] op_sel_hi:[1,0,1] neg_lo:[0,1,0]\n\tv_pk_fma_f32 v[148:149], v[148:149], %[e8], v[152:153] op_sel:[1,1,0] op_sel_hi:[1,0,1] neg_lo:[0,1,0]\n\tds_write_addtid_b32 v157 offset:49536\n\tv_cvt_pk_bf16_f32 v154, v146, v147\n\tv_cvt_pk_bf16_f32 v155, v148, v149\n\tv_pk_mul_f32 v[150:151], %[e8], v[146:147] op_sel_hi:[1,0]\n\tv_pk_mul_f32 v[152:153], %[e8], v[148:149] op_sel_hi:[1,0]\n\tds_write_addtid_b32 v154 offset:20640\n\tv_pk_fma_f32 v[146:147], v[146:147], %[e8], v[150:151] op_sel:[1,1,0] op_sel_hi:[1,0,1] neg_lo:[0,1,0]\n\tv_pk_fma_f32 v[148:149], v[148:149], %[e8], v[152:153] op_sel:[1,1,0] op_sel_hi:[1,0,1] neg_lo:[0,1,0]\n\tds_write_addtid_b32 v155 offset:51600\n\tv_cvt_pk_bf16_f32 v156, v146, v147\n\tv_cvt_pk_bf16_f32 v157, v148, v149\n\tv_pk_mul_f32 v[150:151], %[e8], v[146:147] op_sel_hi:[1,0]\n\tv_pk_mul_f32 v[152:153], %[e8], v[148:149] op_sel_hi:[1,0]\n\tds_write_addtid_b32 v156 offset:22704\n\tv_pk_fma_f32 v[146:147], v[146:147], %[e8], v[150:151] op_sel:[1,1,0] op_sel_hi:[1,0,1] neg_lo:[0,1,0]\n\tv_pk_fma_f32 v[148:149], v[148:149], %[e8], v[152:153] op_sel:[1,1,0] op_sel_hi:[1,0,1] neg_lo:[0,1,0]\n\tds_write_addtid_b32 v157 offset:53664\n\tv_cvt_pk_bf16_f32 v154, v146, v147\n\tv_cvt_pk_bf16_f32 v155, v148, v149\n\tv_pk_mul_f32 v[150:151], %[e8], v[146:147] op_sel_hi:[1,0]\n\tv_pk_mul_f32 v[152:153], %[e8], v[148:149] op_sel_hi:[1,0]\n\tds_write_addtid_b32 v154 offset:24768\n\tv_pk_fma_f32 v[146:147], v[146:147], %[e8], v[150:151] op_sel:[1,1,0] op_sel_hi:[1,0,1] neg_lo:[0,1,0]\n\tv_pk_fma_f32 v[148:149], v[148:149], %[e8], v[152:153] op_sel:[1,1,0] op_sel_hi:[1,0,1] neg_lo:[0,1,0]\n\tds_write_addtid_b32 v155 offset:55728\n\tv_cvt_pk_bf16_f32 v156, v146, v147\n\tv_cvt_pk_bf16_f32 v157, v148, v149\n\tv_pk_mul_f32 v[150:151], %[e8], v[146:147] op_sel_hi:[1,0]\n\tv_pk_mul_f32 v[152:153], %[e8], v[148:149] op_sel_hi:[1,0]\n\tds_write_addtid_b32 v156 offset:26832\n\tv_pk_fma_f32 v[146:147], v[146:147], %[e8], v[150:151] op_sel:[1,1,0] op_sel_hi:[1,0,1] neg_lo:[0,1,0]\n\tv_pk_fma_f32 v[148:149], v[148:149], %[e8], v[152:153] op_sel:[1,1,0] op_sel_hi:[1,0,1] neg_lo:[0,1,0]\n\tds_write_addtid_b32 v157 offset:57792\n\tv_cvt_pk_bf16_f32 v154, v146, v147\n\tv_cvt_pk_bf16_f32 v155, v148, v149\n\tv_pk_mul_f32 v[150:151], %[e8], v[146:147] op_sel_hi:[1,0]\n\tv_pk_mul_f32 v[152:153], %[e8], v[148:149] op_sel_hi:[1,0]\n\tds_write_addtid_b32 v154 offset:28896\n\tv_pk_fma_f32 v[146:147], v[146:147], %[e8], v[150:151] op_sel:[1,1,0] op_sel_hi:[1,0,1] neg_lo:[0,1,0]\n\tv_pk_fma_f32 v[148:149], v[148:149], %[e8], v[152:153] op_sel:[1,1,0] op_sel_hi:[1,0,1] neg_lo:[0,1,0]\n\tds_write_addtid_b32 v155 offset:59856\n\tv_cvt_pk_bf16_f32 v156, v146, v147\n\tv_cvt_pk_bf16_f32 v157, v148, v149\n\tds_write_addtid_b32 v156 offset:30960\n\tds_write_addtid_b32 v157 offset:61920"
        : "+&{v[140:141]}"(T), "+&{v[142:143]}"(U), "+&{v[144:145]}"(WG), "+&{v[146:147]}"(Y), "+&{v[148:149]}"(Y2)
        : [f1] "v"(F1), [f2] "v"(F2), [f4] "v"(F4), [f8] "v"(F8), [g] "v"(G), [e8] "v"(E8),
          [mx] "s"(wave * 256), [my] "s"(wave * 256 + 22 * KG_ROW)
        : "v150", "v151", "v152", "v153", "v154", "v155", "v156", "v157", "memory");
    asm volatile("s_waitcnt lgkmcnt(0)" ::: "memory");
    const LAS char* ra = lds + KG_TAB + wave * 256 + (lane & 31) * KG_ROW + (lane >> 5) * 16;
#pragma unroll
    for (int ks = 0; ks < 8; ++ks) {
        bf16x8 a = *(const LAS bf16x8*)(ra + ks * 32);
        bf16x8 b = *(const LAS bf16x8*)(ra + 21 * KG_ROW + ks * 32);
        acc = mfma32(a, b, acc);
    }
    WAIT_LGKM0();
}

DI void kg_finish(const f32x16& acc, int ch, char* __restrict__ ws, LAS char* lds, int wave, int lane) {
    LAS char* kc = lds + KG_KC + wave * 1312;
    const int b = lane & 31, h = lane >> 5;
    *(LAS u32x2*)(kc + (636 - 8 * b - 4 * h) * 2) = (u32x2){pk2(acc[3], acc[2]), pk2(acc[1], acc[0])};
    *(LAS unsigned*)(kc + (h ? 254 - 2 * b : 382 - 4 * b) * 2) = pk2(acc[5], acc[4]);
    *(LAS unsigned*)(kc + (h ? 190 - 2 * b : 380 - 4 * b) * 2) = pk2(acc[7], acc[6]);
    const unsigned p0 = pk2(acc[8], acc[9]), p1 = pk2(acc[10], acc[11]);
    if (h == 0) {
        *(LAS u16*)(kc + (127 - b) * 2) = (u16)p0;
        *(LAS u16*)(kc + (95 - b) * 2) = (u16)(p0 >> 16);
        *(LAS u16*)(kc + (63 - b) * 2) = (u16)p1;
        *(LAS u16*)(kc + (31 - b) * 2) = (u16)(p1 >> 16);
    } else if (b == 0) {
        *(LAS unsigned*)(kc + 640 * 2) = p0 & 0xffffu;
    }
    WAIT_LGKM0();
    char* dst = ws + WS_KR + (size_t)ch * (KRP * 2) + lane * 16;
#pragma unroll
    for (int i = 0; i < 2; ++i) {
        const unsigned A = *(const LAS u16*)(kc + (lane + 64 * i) * 2), B = *(const LAS u16*)(kc + (lane + 64 * i + 1) * 2);
        const unsigned AA = A | (A << 16), BB = B | (B << 16);
        *(u32x4*)(dst + i * 1024) = (u32x4){AA, AA, ((lane & 1) ? B : A) | (B << 16), BB};
    }
    {
        const unsigned d0 = *(const LAS unsigned*)(kc + (128 + 2 * lane) * 2), d1 = *(const LAS unsigned*)(kc + (130 + 2 * lane) * 2);
        *(u32x4*)(dst + 2048) = (u32x4){__builtin_amdgcn_perm(d0, d0, 0x01000100u), d0, __builtin_amdgcn_perm(d0, d0, 0x03020302u),
                                        __builtin_amdgcn_perm(d1, d1, 0x01000100u)};
    }
    {
        const int j0 = lane < 32 ? 256 + 4 * lane : 384 + 8 * (lane - 32);
        const u32x2 lo = *(const LAS u32x2*)(kc + j0 * 2), hi = *(const LAS u32x2*)(kc + j0 * 2 + 8);
        u32x4 o = {lo[0], lo[1], hi[0], hi[1]};
        if (lane < 32)
            o = (u32x4){__builtin_amdgcn_perm(lo[0], lo[0], 0x01000100u), __builtin_amdgcn_perm(lo[1], lo[0], 0x05040302u),
                        __builtin_amdgcn_perm(lo[1], lo[1], 0x01000100u), __builtin_amdgcn_perm(hi[0], lo[1], 0x05040302u)};
        *(u32x4*)(dst + 3072) = o;
    }
    if (lane < 4) {
        const unsigned k0 = *(const LAS unsigned*)(kc + 640 * 2);
        *(u32x4*)(dst + 4096) = (u32x4){lane == 0 ? k0 : 0u, 0u, 0u, 0u};
    }
    WAIT_LGKM0();
}

DI void k1_kgen(const u16* __restrict__ u, const u16* __restrict__ Are, const u16* __restrict__ Aim, const u16* __restrict__ Bre,
                const u16* __restrict__ Bim, const u16* __restrict__ Cre, const u16* __restrict__ Cim, const u16* __restrict__ logdt,
                const u16* __restrict__ gw, char* __restrict__ ws, LAS char* lds, int kb, int wave, int lane) {
    const int tid = wave * 64 + lane;
    STAMP(0, 0);
    const int chA = 10 * kb + wave, chB = 10 * kb + 8 + ((wave / 3) & 1);
    KgP PA[3], PB;
#pragma unroll
    for (int p = 0; p < 3; ++p) PA[p] = kg_load(Are, Aim, Bre, Bim, Cre, Cim, chA, p, lane);
    PB = kg_load(Are, Aim, Bre, Bim, Cre, Cim, chB, wave % 3, lane);
    const u16 ldtA = logdt[chA], ldtB = logdt[chB];
    {
        __amdgpu_buffer_rsrc_t ru = __builtin_amdgcn_make_buffer_rsrc((void*)u, 0, L * D * 2, 0x00020000);
#pragma unroll
        for (int i = 0; i < 5; ++i) {
            const int id = (wave * 5 + i) * 64 + lane;
            const int row = (id * 52429) >> 22, c = id - 80 * row;
            const int f = (row & 3) | ((row >> 1) & 4);
            __builtin_amdgcn_raw_ptr_buffer_load_lds(ru, (LAS void*)(lds + KG_UT + (wave * 5 + i) * 1024), 16,
                                                     (32 * kb + row) * (D * 2) + (c ^ (f << 1)) * 16, 0, 0, 0);
        }
        if (wave < 2) {
            __amdgpu_buffer_rsrc_t rg = __builtin_amdgcn_make_buffer_rsrc((void*)gw, 0, D * 2, 0x00020000);
            __builtin_amdgcn_raw_ptr_buffer_load_lds(rg, (LAS void*)(lds + KG_GW + wave * 1024), 16, wave * 1024 + lane * 16, 0, 0, 0);
        }
    }
    STAMP(0, 1);
    {
        const float dt = rbf(expf(bf2f(ldtA)));
        f32x16 acc;
#pragma unroll
        for (int i = 0; i < 16; ++i) acc[i] = 0.f;
        KgP P0 = PA[0], P1 = PA[1], P2 = PA[2];
#pragma unroll 1
        for (int pass = 0; pass < 3; ++pass) { kg_pass(P0, dt, lds, wave, lane, acc); P0 = P1; P1 = P2; }
        STAMP(0, 2);
        kg_finish(acc, chA, ws, lds, wave, lane);
    }
    STAMP(0, 3);
    {
        const int pass = wave % 3;
        f32x16 acc;
#pragma unroll
        for (int i = 0; i < 16; ++i) acc[i] = 0.f;
        if (wave < 6) {
            const float dt = rbf(expf(bf2f(ldtB)));
            kg_pass(PB, dt, lds, wave, lane, acc);
            if (pass != 0) {
                LAS char* tb = lds + KG_TAB + wave * 256 + lane * 4;
#pragma unroll
                for (int i = 0; i < 12; ++i) *(LAS float*)(tb + i * KG_ROW) = acc[i];
            }
        }
        WAIT_VM(0);
        __syncthreads();
        if (wave < 6 && pass == 0) {
#pragma unroll
            for (int o = 1; o < 3; ++o) {
                const LAS char* tb = lds + KG_TAB + (wave + o) * 256 + lane * 4;
#pragma unroll
                for (int i = 0; i < 12; ++i) acc[i] += *(const LAS float*)(tb + i * KG_ROW);
            }
            kg_finish(acc, chB, ws, lds, wave, lane);
        }
    }
    STAMP(0, 4);
    {
        const int row = tid >> 4, l = tid & 15;
        const int f = (row & 3) | ((row >> 1) & 4);
        float s = 0.f;
#pragma unroll
        for (int i = 0; i < 5; ++i) {
            const int c = l + 16 * i;
            const u32x4 v = *(const LAS u32x4*)(lds + KG_UT + row * 1280 + (c ^ (f << 1)) * 16);
            const u32x4 gv = *(const LAS u32x4*)(lds + KG_GW + c * 16);
#pragma unroll
            for (int j = 0; j < 4; ++j) {
                const unsigned a = v[j], b = gv[j];
                s = __builtin_amdgcn_fdot2_f32_bf16(__builtin_bit_cast(bf16x2, a), __builtin_bit_cast(bf16x2, b), s, false);
            }
        }
        s += __shfl_xor(s, 1); s += __shfl_xor(s, 2); s += __shfl_xor(s, 4); s += __shfl_xor(s, 8);
        if (l == 0) {
            const float sb = rbf(s);
            ((float*)(ws + WS_G))[32 * kb + row] = rbf(1.f / (1.f + __expf(-sb)));
        }
        const int g = lane >> 4, q = (lane & 15) >> 2, p = lane & 3;
        const int r0 = 8 * g + q, r1 = r0 + 4;
        const int f0 = (r0 & 3) | ((r0 >> 1) & 4);
#pragma unroll
        for (int i = 0; i < 5; ++i) {
            const int ct = wave * 5 + i;
            const int c = ((2 * ct + (p >> 1)) ^ (f0 << 1)) * 16 + (p & 1) * 8;
            bf16x8 fr = lds_frag_tr(lds + KG_UT + r0 * 1280 + c, lds + KG_UT + r1 * 1280 + c);
            *(bf16x8*)(ws + WS_UT + ((size_t)(16 * ct + (lane & 15)) * L + 32 * kb + 8 * g) * 2) = fr;
        }
    }
    STAMP(0, 5);
}

__global__ __launch_bounds__(512, 2) void k1_kernel(
    const u16* __restrict__ u, const u16* __restrict__ Are, const u16* __restrict__ Aim, const u16* __restrict__ Bre,
    const u16* __restrict__ Bim, const u16* __restrict__ Cre, const u16* __restrict__ Cim, const u16* __restrict__ logdt,
    const u16* __restrict__ wqkv, const u16* __restrict__ gw, char* __restrict__ ws) {
    extern __shared__ __attribute__((aligned(16))) char smem[];
    LAS char* lds = (LAS char*)smem;
    const int wave = __builtin_amdgcn_readfirstlane(threadIdx.x >> 6), lane = threadIdx.x & 63;
    const int xcd = blockIdx.x & 7, j = blockIdx.x >> 3;
    const int xr = xcd & 3, xc = xcd >> 2;
    const int ra = (0x8530 >> (4 * (j & 3))) & 15;
    const int rc = (0x875320 >> (4 * (j >> 2))) & 15;
    const int rot = 0;
    if (j < 16) k1_gemm<false>(u, wqkv, ws, lds, 4 * xr + (j & 3), 4 * xc + (j >> 2), rot, wave, lane);
    else if (j < 24) k1_gemm<true>(u, wqkv, ws, lds, 4 * xr + (j & 3), 2 * xc + ((j - 16) >> 2), rot, wave, lane);
    else k1_kgen(u, Are, Aim, Bre, Bim, Cre, Cim, logdt, gw, ws, lds, (j - 24) * 8 + xcd, wave, lane);
}

constexpr int CV_U = 0;
constexpr int CV_R = 4608;
constexpr int CV_RED = CV_R;
constexpr int K2_LDS = CV_R + 24576;

DI void k2_conv(const u16* __restrict__ dskip, char* __restrict__ ws, LAS char* lds, int ch, int wave, int lane) {
    const int tid = wave * 64 + lane;
    STAMP(1, 0);
    const char* kr = ws + WS_KR + (size_t)ch * (KRP * 2);
    const char* ut = ws + WS_UT + (size_t)ch * (L * 2);
    {
        const u32x4 uv = *(const u32x4*)(ut + tid * 16);
        auto copies = [&](int y) {
            const u32x4 a = *(const u32x4*)(kr + y * 16);
            const u32x2 b = *(const u32x2*)(kr + y * 16 + 16);
            const unsigned s0 = __builtin_amdgcn_alignbyte(a[1], a[0], 2), s1 = __builtin_amdgcn_alignbyte(a[2], a[1], 2),
                           s2 = __builtin_amdgcn_alignbyte(a[3], a[2], 2), s3 = __builtin_amdgcn_alignbyte(b[0], a[3], 2),
                           s4 = __builtin_amdgcn_alignbyte(b[1], b[0], 2);
            LAS char* dst = lds + CV_R + y * 16;
            *(LAS u32x4*)(dst) = a;
            *(LAS u32x4*)(dst + 4160) = (u32x4){s0, s1, s2, s3};
            *(LAS u32x4*)(dst + 2 * 4160) = (u32x4){a[1], a[2], a[3], b[0]};
            *(LAS u32x4*)(dst + 3 * 4160) = (u32x4){s1, s2, s3, s4};
        };
        copies(tid);
        if (tid < 2) copies(256 + tid);
        *(LAS u32x4*)(lds + CV_U + 512 + tid * 16) = uv;
        if (tid < 32) *(LAS u32x4*)(lds + CV_U + tid * 16) = (u32x4){0, 0, 0, 0};
    }
    __syncthreads();
    STAMP(1, 1);
    const int r = lane & 15, g = lane >> 4;
    f32x4 acc[8];
#pragma unroll
    for (int c = 0; c < 8; ++c) acc[c] = (f32x4){0.f, 0.f, 0.f, 0.f};
    {
        const int p = (-r) & 3;
        const int abase = CV_R + p * 4160 + 2 * (2032 - r - p + 8 * g) - 512 * 7;
        const int bbase = CV_U + 2 * (240 + 16 * r + 8 * g);
#pragma unroll 1
        for (int rr = 0; rr < 2; ++rr) {
            const int rho = 2 * wave + rr;
            const LAS char* pa = lds + abase - 64 * rho;
            const LAS char* pb = lds + bbase - 64 * rho;
            bf16x8 fb[8];
#pragma unroll
            for (int b = 0; b < 8; ++b) fb[b] = *(const LAS bf16x8*)(pb + 512 * b);
#pragma unroll
            for (int a = 0; a < 8; ++a) {
                const u32x2 lo = *(const LAS u32x2*)(pa + 512 * (7 - a));
                const u32x2 hi = *(const LAS u32x2*)(pa + 512 * (7 - a) + 8);
                const bf16x8 fa = asfrag((u32x4){lo[0], lo[1], hi[0], hi[1]});
#pragma unroll
                for (int b = 0; b + a < 8; ++b) acc[a + b] = mfma16(fa, fb[b], acc[a + b]);
            }
        }
    }
    STAMP(1, 2);
    __syncthreads();
    f32x4 o0, o1;
#pragma unroll
    for (int c = 0; c < 8; ++c) {
        if ((c >> 1) != wave) {
            const int k = c - (c > 2 * wave + 1 ? 2 : 0);
            *(LAS f32x4*)(lds + CV_RED + (wave * 6 + k) * 1024 + lane * 16) = acc[c];
        } else if (c & 1) o1 = acc[c];
        else o0 = acc[c];
    }
    __syncthreads();
#pragma unroll
    for (int s = 0; s < 4; ++s) {
        if (s != wave) {
            const int k = 2 * wave - (wave > s ? 2 : 0);
            o0 += *(const LAS f32x4*)(lds + CV_RED + (s * 6 + k) * 1024 + lane * 16);
            o1 += *(const LAS f32x4*)(lds + CV_RED + (s * 6 + k + 1) * 1024 + lane * 16);
        }
    }
    const float ds = bf2f(dskip[ch]);
#pragma unroll
    for (int i = 0; i < 2; ++i) {
        const f32x4 o = i ? o1 : o0;
        const int t = 16 * (16 * (2 * wave + i) + r) + 4 * g;
        const u32x2 uu = *(const LAS u32x2*)(lds + CV_U + 512 + t * 2);
        const float a0 = rbf(o[0]) + rbf(bflo(uu[0]) * ds), a1 = rbf(o[1]) + rbf(bfhi(uu[0]) * ds);
        const float a2 = rbf(o[2]) + rbf(bflo(uu[1]) * ds), a3 = rbf(o[3]) + rbf(bfhi(uu[1]) * ds);
        *(u32x2*)(ws + WS_AT + ((size_t)ch * L + t) * 2) = (u32x2){pk2(a0, a1), pk2(a2, a3)};
    }
    STAMP(1, 3);
}

DI void k2_mprod(const u16* __restrict__ wo, char* __restrict__ ws, LAS char* lds, int h, int cg, int wave, int lane) {
    const int tid = wave * 64 + lane;
    STAMP(1, 0);
    LAS char* kvb = lds;
    LAS char* wos = lds + 4096;
    for (int i = tid; i < 320; i += 256) {
        const int e = i / 10, c = i % 10;
        u32x4 v = (u32x4){0, 0, 0, 0};
        if (20 * h + e < D) v = *(const u32x4*)(wo + (size_t)(20 * h + e) * D + 80 * cg + 8 * c);
        *(LAS u32x4*)(wos + e * 160 + c * 16) = v;
    }
    for (int i = tid; i < 32 * 20; i += 256) ((LAS unsigned*)kvb)[i] = 0;
    __syncthreads();
    if (tid < 100) {
        const float* src = (const float*)(ws + WS_KVP) + (size_t)h * 400 + tid * 4;
        f32x4 v[16];
#pragma unroll
        for (int p = 0; p < 16; ++p) v[p] = *(const f32x4*)(src + (size_t)p * 32 * 400);
        f32x4 s = v[0];
#pragma unroll
        for (int p = 1; p < 16; ++p) s += v[p];
        const int d = tid / 5, e0 = (tid % 5) * 4;
        *(LAS u32x2*)(kvb + d * 80 + e0 * 2) = (u32x2){pk2(s[0], s[1]), pk2(s[2], s[3])};
    }
    __syncthreads();
    const int l15 = lane & 15, g = lane >> 4, q = l15 >> 2, p = lane & 3;
    for (int ct = wave; ct < 5; ct += 4) {
        const LAS char* p0 = wos + (8 * g + q) * 160 + ct * 32 + p * 8;
        const bf16x8 fb = lds_frag_tr(p0, p0 + 4 * 160);
#pragma unroll
        for (int dt = 0; dt < 2; ++dt) {
            const bf16x8 fa = *(const LAS bf16x8*)(kvb + (16 * dt + l15) * 80 + g * 16);
            f32x4 dd = mfma16(fa, fb, (f32x4){0.f, 0.f, 0.f, 0.f});
            const int gl = 4 * dt + g;
            if (gl < 5) {
                const int G = 5 * h + gl;
                const int s = G >> 3, half = (G >> 2) & 1, gg = G & 3;
                *(u32x2*)(ws + WS_MF + ((size_t)((cg * 5 + ct) * 20 + s) * 64 + l15 + 16 * gg) * 16 + half * 8) =
                    (u32x2){pk2(dd[0], dd[1]), pk2(dd[2], dd[3])};
            }
        }
    }
    STAMP(1, 3);
}

DI void k2_norm(char* __restrict__ ws, LAS char* lds, int rg, int j, int wave, int lane) {
    const int tid = wave * 64 + lane;
    STAMP(1, 0);
    const int rt = 4 * rg + wave;
    const char* src = ws + WS_PQF + ((size_t)(rt * 20 + 5 * j) * 64 + lane) * 16;
    u32x4 f[5];
#pragma unroll
    for (int s = 0; s < 5; ++s) f[s] = *(const u32x4*)(src + s * 1024);
    if (tid < 160) {
        const float* zs = (const float*)(ws + WS_ZP) + 160 * j + tid;
        float v[16];
#pragma unroll
        for (int p = 0; p < 16; ++p) v[p] = zs[p * 640];
        float s = v[0];
#pragma unroll
        for (int p = 1; p < 16; ++p) s += v[p];
        ((LAS u16*)lds)[tid] = (u16)pk2(s, s);
    }
    __syncthreads();
    const int l15 = lane & 15, g = lane >> 4;
    f32x4 den = {0.f, 0.f, 0.f, 0.f};
#pragma unroll
    for (int s = 0; s < 5; ++s) {
        const int n0 = 32 * s + 4 * g, n1 = n0 + 16;
        u32x2 z0 = *(const LAS u32x2*)(lds + n0 * 2), z1 = *(const LAS u32x2*)(lds + n1 * 2);
        if (n0 / 20 != l15) z0 = (u32x2){0, 0};
        if (n1 / 20 != l15) z1 = (u32x2){0, 0};
        den = mfma16(asfrag((u32x4){z0[0], z0[1], z1[0], z1[1]}), asfrag(f[s]), den);
    }
    LAS float* rinv = (LAS float*)(lds + 512) + wave * 16 * 12;
    if (g < 2) {
        f32x4 rv;
#pragma unroll
        for (int r = 0; r < 4; ++r) rv[r] = 1.f / rbf(rbf(den[r]) + 1e-6f);
        *(LAS f32x4*)(rinv + l15 * 12 + 4 * g) = rv;
    }
    WAIT_LGKM0();
    char* dst = ws + WS_PQN + ((size_t)(rt * 20 + 5 * j) * 64 + lane) * 16;
#pragma unroll
    for (int s = 0; s < 5; ++s) {
        const float r0 = rinv[l15 * 12 + (32 * s + 4 * g) / 20], r1 = rinv[l15 * 12 + (32 * s + 16 + 4 * g) / 20];
        u32x4 o;
        o[0] = pk2(bflo(f[s][0]) * r0, bfhi(f[s][0]) * r0);
        o[1] = pk2(bflo(f[s][1]) * r0, bfhi(f[s][1]) * r0);
        o[2] = pk2(bflo(f[s][2]) * r1, bfhi(f[s][2]) * r1);
        o[3] = pk2(bflo(f[s][3]) * r1, bfhi(f[s][3]) * r1);
        *(u32x4*)(dst + s * 1024) = o;
    }
    STAMP(1, 3);
}

__global__ __launch_bounds__(256, 4) void k2_kernel(const u16* __restrict__ dskip, const u16* __restrict__ wo, char* __restrict__ ws) {
    extern __shared__ __attribute__((aligned(16))) char smem[];
    LAS char* lds = (LAS char*)smem;
    const int wave = __builtin_amdgcn_readfirstlane(threadIdx.x >> 6), lane = threadIdx.x & 63;
    const int b = blockIdx.x;
    if (b < 128) k2_norm(ws, lds, b >> 2, b & 3, wave, lane);
    else if (b < 384) k2_mprod(wo, ws, lds, (b - 128) >> 3, (b - 128) & 7, wave, lane);
    else k2_conv(dskip, ws, lds, b - 384, wave, lane);
}

constexpr int K3_M = 0;
constexpr int K3_A = 102400;
constexpr int K3_LDS = K3_A + 10240;

__global__ __launch_bounds__(256, 1) void k3_kernel(char* __restrict__ ws, u16* __restrict__ out) {
    extern __shared__ __attribute__((aligned(16))) char smem[];
    LAS char* lds = (LAS char*)smem;
    const int wave = __builtin_amdgcn_readfirstlane(threadIdx.x >> 6), lane = threadIdx.x & 63;
    const int rb = (blockIdx.x & 7) + 8 * ((blockIdx.x >> 3) & 3), cg = blockIdx.x >> 5;
    const int l15 = lane & 15, g = lane >> 4;
    STAMP(2, 0);
    __amdgpu_buffer_rsrc_t rm = __builtin_amdgcn_make_buffer_rsrc((void*)(ws + WS_MF), 0, 819200, 0x00020000);
    __amdgpu_buffer_rsrc_t ra = __builtin_amdgcn_make_buffer_rsrc((void*)(ws + WS_AT), 0, L * D * 2, 0x00020000);
#pragma unroll
    for (int i = 0; i < 25; ++i)
        __builtin_amdgcn_raw_ptr_buffer_load_lds(rm, (LAS void*)(lds + K3_M + (wave * 25 + i) * 1024), 16, lane * 16,
                                                 cg * 102400 + (wave * 25 + i) * 1024, 0, 0);
    for (int i = wave; i < 10; i += 4)
        __builtin_amdgcn_raw_ptr_buffer_load_lds(ra, (LAS void*)(lds + K3_A + i * 1024), 16,
                                                 ((80 * cg + 8 * i + (lane >> 3)) * L + 64 * rb) * 2 + (lane & 7) * 16, 0, 0, 0);
    const int rt = 4 * rb + wave;
    const char* src = ws + WS_PQN + ((size_t)(rt * 20) * 64 + lane) * 16;
    u32x4 f[20];
#pragma unroll
    for (int s = 0; s < 20; ++s) f[s] = *(const u32x4*)(src + s * 1024);
    const float gt = ((const float*)(ws + WS_G))[16 * rt + l15];
    WAIT_VM(0);
    BAR();
    STAMP(2, 1);
    f32x4 acc[5];
#pragma unroll
    for (int c = 0; c < 5; ++c) acc[c] = (f32x4){0.f, 0.f, 0.f, 0.f};
#pragma unroll
    for (int s = 0; s < 20; ++s)
#pragma unroll
        for (int c = 0; c < 5; ++c) {
            const bf16x8 fm = *(const LAS bf16x8*)(lds + K3_M + (c * 20 + s) * 1024 + lane * 16);
            acc[c] = mfma16(fm, asfrag(f[s]), acc[c]);
        }
    STAMP(2, 2);
    const float og = rbf(1.f - gt);
    const int q = l15 >> 2, p = lane & 3;
#pragma unroll
    for (int c = 0; c < 5; ++c) {
        const s16x4 av = __builtin_amdgcn_ds_read_tr16_b64_v4i16(
            (LAS s16x4*)(lds + K3_A + (16 * c + 4 * g + q) * 128 + (16 * wave + 4 * p) * 2));
        const u32x2 aa = __builtin_bit_cast(u32x2, av);
        const float a0 = bflo(aa[0]), a1 = bfhi(aa[0]), a2 = bflo(aa[1]), a3 = bfhi(aa[1]);
        const float o0 = rbf(gt * a0) + rbf(og * rbf(acc[c][0])), o1 = rbf(gt * a1) + rbf(og * rbf(acc[c][1]));
        const float o2 = rbf(gt * a2) + rbf(og * rbf(acc[c][2])), o3 = rbf(gt * a3) + rbf(og * rbf(acc[c][3]));
        *(u32x2*)(out + (size_t)(16 * rt + l15) * D + 80 * cg + 16 * c + 4 * g) = (u32x2){pk2(o0, o1), pk2(o2, o3)};
    }
    STAMP(2, 3);
}

extern "C" void kernel_launch(void* const* d_in, const int* in_sizes, int n_in, void* d_out, int out_size, void* d_ws,
                              size_t ws_size, hipStream_t stream) {
    const u16* u = (const u16*)d_in[0];
    const u16* Are = (const u16*)d_in[1];
    const u16* Aim = (const u16*)d_in[2];
    const u16* Bre = (const u16*)d_in[3];
    const u16* Bim = (const u16*)d_in[4];
    const u16* Cre = (const u16*)d_in[5];
    const u16* Cim = (const u16*)d_in[6];
    const u16* logdt = (const u16*)d_in[7];
    const u16* dskip = (const u16*)d_in[8];
    const u16* wqkv = (const u16*)d_in[10];
    const u16* wo = (const u16*)d_in[11];
    const u16* gw = (const u16*)d_in[12];
    char* ws = (char*)d_ws;
    (void)hipFuncSetAttribute((const void*)k1_kernel, hipFuncAttributeMaxDynamicSharedMemorySize, K1_LDS_ALL);
    (void)hipFuncSetAttribute((const void*)k3_kernel, hipFuncAttributeMaxDynamicSharedMemorySize, K3_LDS);
    k1_kernel<<<256, 512, K1_LDS_ALL, stream>>>(u, Are, Aim, Bre, Bim, Cre, Cim, logdt, wqkv, gw, ws);
    k2_kernel<<<1024, 256, K2_LDS, stream>>>(dskip, wo, ws);
    k3_kernel<<<256, 256, K3_LDS, stream>>>(ws, (u16*)d_out);
}
```

```cpp
#include <hip/hip_runtime.h>
#include <hip/hip_bf16.h>
#include <stdint.h>


typedef __bf16 bf16x8 __attribute__((ext_vector_type(8)));
typedef __bf16 bf16x2 __attribute__((ext_vector_type(2)));
typedef short s16x4 __attribute__((ext_vector_type(4)));
typedef short s16x8 __attribute__((ext_vector_type(8)));
typedef float f32x2 __attribute__((ext_vector_type(2)));
typedef float f32x4 __attribute__((ext_vector_type(4)));
typedef float f32x16 __attribute__((ext_vector_type(16)));
typedef unsigned u32x2 __attribute__((ext_vector_type(2)));
typedef unsigned u32x4 __attribute__((ext_vector_type(4)));
typedef unsigned short u16;
#define LAS __attribute__((address_space(3)))
#define DI __device__ __forceinline__

constexpr int L = 2048, D = 640, NM = 192, NH = 32, DH = 20;
constexpr size_t WS_PQF = 0;
constexpr size_t WS_KVP = WS_PQF + 2621440;
constexpr size_t WS_MF  = WS_KVP + 860160;
constexpr size_t WS_ZB  = WS_MF + 819200;
constexpr size_t WS_CH  = WS_ZB + 10240;
constexpr int CHREC = 4096 + 1344;
constexpr size_t WS_AT  = WS_CH + 640 * CHREC;
constexpr size_t WS_G   = WS_AT + 2621440;
constexpr size_t WS_CNT = WS_G + 8192;
constexpr size_t WS_END = WS_CNT + 4096 + 8192;

DI unsigned pk2(float lo, float hi) { f32x2 v = {lo, hi}; return __builtin_bit_cast(unsigned, __builtin_convertvector(v, bf16x2)); }
DI float bflo(unsigned u) { return __uint_as_float(u << 16); }
DI float bfhi(unsigned u) { return __uint_as_float(u & 0xffff0000u); }
DI float bf2f(u16 h) { return __uint_as_float((unsigned)h << 16); }
DI float rbf(float x) { return bflo(pk2(x, x)); }
DI bf16x8 asfrag(u32x4 v) { return __builtin_bit_cast(bf16x8, v); }
DI f32x4 mfma16(bf16x8 a, bf16x8 b, f32x4 c) { return __builtin_amdgcn_mfma_f32_16x16x32_bf16(a, b, c, 0, 0, 0); }
DI f32x16 mfma32(bf16x8 a, bf16x8 b, f32x16 c) { return __builtin_amdgcn_mfma_f32_32x32x16_bf16(a, b, c, 0, 0, 0); }
DI bf16x8 lds_frag(const char* p) { return *(const LAS bf16x8*)(const LAS char*)p; }
DI bf16x8 lds_frag_tr(const LAS char* p0, const LAS char* p1) {
    s16x4 a = __builtin_amdgcn_ds_read_tr16_b64_v4i16((LAS s16x4*)p0);
    s16x4 b = __builtin_amdgcn_ds_read_tr16_b64_v4i16((LAS s16x4*)p1);
    s16x8 ab = __builtin_shufflevector(a, b, 0, 1, 2, 3, 4, 5, 6, 7);
    return __builtin_bit_cast(bf16x8, ab);
}
#define WAIT_VM(n) do { asm volatile("" ::: "memory"); __builtin_amdgcn_s_waitcnt(0x0F70 | ((n) & 15) | (((n) >> 4) << 14)); asm volatile("" ::: "memory"); } while (0)
typedef __amdgpu_buffer_rsrc_t rsrc_t;
DI void st16w(rsrc_t r, unsigned off, u32x4 v) { __builtin_amdgcn_raw_buffer_store_b128(v, r, off, 0, 16); }
DI void st8w(rsrc_t r, unsigned off, u32x2 v) { __builtin_amdgcn_raw_buffer_store_b64(v, r, off, 0, 16); }
DI void st4w(rsrc_t r, unsigned off, unsigned v) { __builtin_amdgcn_raw_buffer_store_b32(v, r, off, 0, 16); }
DI rsrc_t ws_rsrc(char* ws) { return __builtin_amdgcn_make_buffer_rsrc((void*)ws, 0, (int)WS_END, 0x00020000); }
#define WAIT_LGKM0() do { __builtin_amdgcn_s_waitcnt(0xC07F); asm volatile("" ::: "memory"); } while (0)
#define BAR() __builtin_amdgcn_s_barrier()

constexpr float LOG2E = 1.4426950408889634f;
DI f32x2 phi2(f32x2 q) {
    const f32x2 a = q * LOG2E, b = q + 1.f;
    const f32x2 m = {fmaxf(__builtin_amdgcn_fmed3f(__builtin_amdgcn_exp2f(a[0]), 0.f, 1.f), b[0]),
                     fmaxf(__builtin_amdgcn_fmed3f(__builtin_amdgcn_exp2f(a[1]), 0.f, 1.f), b[1])};
    return m + 1e-6f;
}
DI unsigned pk2f(f32x2 v) { return __builtin_bit_cast(unsigned, __builtin_convertvector(v, bf16x2)); }
DI f32x2 lo2(f32x4 v) { return __builtin_shufflevector(v, v, 0, 1); }
DI f32x2 hi2(f32x4 v) { return __builtin_shufflevector(v, v, 2, 3); }

typedef f32x2 cf;
DI cf rot(cf b) { return (cf){-b[1], b[0]}; }
DI cf cmul(cf a, cf b) {
    const cf t = __builtin_shufflevector(a, a, 0, 0) * b;
    return __builtin_elementwise_fma(__builtin_shufflevector(a, a, 1, 1), rot(b), t);
}
DI cf csq(cf a) { return (cf){__builtin_fmaf(a[0], a[0], -a[1] * a[1]), (a[0] + a[0]) * a[1]}; }
DI cf cexp_(float zr, float zi_rev, float c) {
    const float m = __builtin_amdgcn_exp2f(zr * c);
    const float a = __builtin_amdgcn_fractf(zi_rev * c);
    return (cf){m * __builtin_amdgcn_cosf(a), m * __builtin_amdgcn_sinf(a)};
}
struct KgP { u16 are, aim, bre, bim, cre, cim; };
struct KgIn { const u16 *Are, *Aim, *Bre, *Bim, *Cre, *Cim, *logdt; };
DI KgP kg_load(const KgIn& in, int ch, int pass, int lane) {
    const int i = ch * NM + pass * 64 + lane;
    KgP p; p.are = in.Are[i]; p.aim = in.Aim[i]; p.bre = in.Bre[i]; p.bim = in.Bim[i]; p.cre = in.Cre[i]; p.cim = in.Cim[i];
    return p;
}
DI float kg_dt(u16 logdt) { return rbf(expf(bf2f(logdt))); }

#define KG_CHAINS_1808 \
    "s_mov_b32 m0, %[mx]\n\tv_cvt_pk_bf16_f32 v154, v140, v141\n\t" \
    "v_cvt_pk_bf16_f32 v155, v146, v147\n\tv_mov_b32 v157, 0x3f80\n\t" \
    "ds_write_addtid_b32 v157 offset:37968\n\tv_pk_mul_f32 v[150:151], %[f1], v[140:141] op_sel_hi:[1,0]\n\t" \
    "v_pk_mul_f32 v[152:153], %[f2], v[142:143] op_sel_hi:[1,0]\n\tds_write_addtid_b32 v154 offset:36160\n\t" \
    "v_pk_fma_f32 v[140:141], v[140:141], %[f1], v[150:151] op_sel:[1,1,0] op_sel_hi:[1,0,1] neg_lo:[0,1,0]\n\tv_pk_fma_f32 v[142:143], v[142:143], %[f2], v[152:153] op_sel:[1,1,0] op_sel_hi:[1,0,1] neg_lo:[0,1,0]\n\t" \
    "ds_write_addtid_b32 v155 offset:39776\n\tv_cvt_pk_bf16_f32 v156, v140, v141\n\t" \
    "v_cvt_pk_bf16_f32 v157, v142, v143\n\tv_pk_mul_f32 v[150:151], %[f1], v[140:141] op_sel_hi:[1,0]\n\t" \
    "v_pk_mul_f32 v[152:153], %[f2], v[142:143] op_sel_hi:[1,0]\n\tds_write_addtid_b32 v156 offset:0\n\t" \
    "v_pk_fma_f32 v[140:141], v[140:141], %[f1], v[150:151] op_sel:[1,1,0] op_sel_hi:[1,0,1] neg_lo:[0,1,0]\n\tv_pk_fma_f32 v[142:143], v[142:143], %[f2], v[152:153] op_sel:[1,1,0] op_sel_hi:[1,0,1] neg_lo:[0,1,0]\n\t" \
    "ds_write_addtid_b32 v157 offset:14464\n\tv_cvt_pk_bf16_f32 v154, v140, v141\n\t" \
    "v_cvt_pk_bf16_f32 v155, v142, v143\n\tv_pk_mul_f32 v[150:151], %[f1], v[140:141] op_sel_hi:[1,0]\n\t" \
    "v_pk_mul_f32 v[152:153], %[f2], v[142:143] op_sel_hi:[1,0]\n\tds_write_addtid_b32 v154 offset:1808\n\t" \
    "v_pk_fma_f32 v[140:141], v[140:141], %[f1], v[150:151] op_sel:[1,1,0] op_sel_hi:[1,0,1] neg_lo:[0,1,0]\n\tv_pk_fma_f32 v[142:143], v[142:143], %[f2], v[152:153] op_sel:[1,1,0] op_sel_hi:[1,0,1] neg_lo:[0,1,0]\n\t" \
    "ds_write_addtid_b32 v155 offset:16272\n\tv_cvt_pk_bf16_f32 v156, v140, v141\n\t" \
    "v_cvt_pk_bf16_f32 v157, v142, v143\n\tv_pk_mul_f32 v[150:151], %[f1], v[140:141] op_sel_hi:[1,0]\n\t" \
    "v_pk_mul_f32 v[152:153], %[f2], v[142:143] op_sel_hi:[1,0]\n\tds_write_addtid_b32 v156 offset:3616\n\t" \
    "v_pk_fma_f32 v[140:141], v[140:141], %[f1], v[150:151] op_sel:[1,1,0] op_sel_hi:[1,0,1] neg_lo:[0,1,0]\n\tv_pk_fma_f32 v[142:143], v[142:143], %[f2], v[152:153] op_sel:[1,1,0] op_sel_hi:[1,0,1] neg_lo:[0,1,0]\n\t" \
    "ds_write_addtid_b32 v157 offset:18080\n\tv_cvt_pk_bf16_f32 v154, v140, v141\n\t" \
    "v_cvt_pk_bf16_f32 v155, v142, v143\n\tv_pk_mul_f32 v[150:151], %[f1], v[140:141] op_sel_hi:[1,0]\n\t" \
    "v_pk_mul_f32 v[152:153], %[g], v[144:145] op_sel_hi:[1,0]\n\tds_write_addtid_b32 v154 offset:5424\n\t" \
    "v_pk_fma_f32 v[140:141], v[140:141], %[f1], v[150:151] op_sel:[1,1,0] op_sel_hi:[1,0,1] neg_lo:[0,1,0]\n\tv_pk_fma_f32 v[144:145], v[144:145], %[g], v[152:153] op_sel:[1,1,0] op_sel_hi:[1,0,1] neg_lo:[0,1,0]\n\t" \
    "ds_write_addtid_b32 v155 offset:19888\n\tv_cvt_pk_bf16_f32 v156, v140, v141\n\t" \
    "v_pk_mul_f32 v[150:151], %[f1], v[140:141] op_sel_hi:[1,0]\n\tv_pk_mul_f32 v[152:153], %[f4], v[144:145] op_sel_hi:[1,0]\n\t" \
    "ds_write_addtid_b32 v156 offset:7232\n\tv_pk_fma_f32 v[140:141], v[140:141], %[f1], v[150:151] op_sel:[1,1,0] op_sel_hi:[1,0,1] neg_lo:[0,1,0]\n\t" \
    "v_pk_fma_f32 v[142:143], v[144:145], %[f4], v[152:153] op_sel:[1,1,0] op_sel_hi:[1,0,1] neg_lo:[0,1,0]\n\tv_cvt_pk_bf16_f32 v157, v140, v141\n\t" \
    "v_cvt_pk_bf16_f32 v154, v142, v143\n\tv_pk_mul_f32 v[150:151], %[f1], v[140:141] op_sel_hi:[1,0]\n\t" \
    "v_pk_mul_f32 v[152:153], %[f4], v[142:143] op_sel_hi:[1,0]\n\tds_write_addtid_b32 v157 offset:9040\n\t" \
    "v_pk_fma_f32 v[140:141], v[140:141], %[f1], v[150:151] op_sel:[1,1,0] op_sel_hi:[1,0,1] neg_lo:[0,1,0]\n\tv_pk_fma_f32 v[142:143], v[142:143], %[f4], v[152:153] op_sel:[1,1,0] op_sel_hi:[1,0,1] neg_lo:[0,1,0]\n\t" \
    "ds_write_addtid_b32 v154 offset:21696\n\tv_cvt_pk_bf16_f32 v155, v140, v141\n\t" \
    "v_cvt_pk_bf16_f32 v156, v142, v143\n\tv_pk_mul_f32 v[150:151], %[f1], v[140:141] op_sel_hi:[1,0]\n\t" \
    "v_pk_mul_f32 v[152:153], %[g], v[144:145] op_sel_hi:[1,0]\n\tds_write_addtid_b32 v155 offset:10848\n\t" \
    "v_pk_fma_f32 v[140:141], v[140:141], %[f1], v[150:151] op_sel:[1,1,0] op_sel_hi:[1,0,1] neg_lo:[0,1,0]\n\tv_pk_fma_f32 v[144:145], v[144:145], %[g], v[152:153] op_sel:[1,1,0] op_sel_hi:[1,0,1] neg_lo:[0,1,0]\n\t" \
    "ds_write_addtid_b32 v156 offset:23504\n\tv_cvt_pk_bf16_f32 v157, v140, v141\n\t" \
    "v_pk_mul_f32 v[150:151], %[f4], v[144:145] op_sel_hi:[1,0]\n\tv_pk_mul_f32 v[152:153], %[g], v[144:145] op_sel_hi:[1,0]\n\t" \
    "ds_write_addtid_b32 v157 offset:12656\n\tv_pk_fma_f32 v[142:143], v[144:145], %[f4], v[150:151] op_sel:[1,1,0] op_sel_hi:[1,0,1] neg_lo:[0,1,0]\n\t" \
    "v_pk_fma_f32 v[144:145], v[144:145], %[g], v[152:153] op_sel:[1,1,0] op_sel_hi:[1,0,1] neg_lo:[0,1,0]\n\tv_cvt_pk_bf16_f32 v154, v142, v143\n\t" \
    "v_pk_mul_f32 v[150:151], %[f4], v[142:143] op_sel_hi:[1,0]\n\tv_pk_mul_f32 v[152:153], %[f8], v[144:145] op_sel_hi:[1,0]\n\t" \
    "ds_write_addtid_b32 v154 offset:25312\n\tv_pk_fma_f32 v[142:143], v[142:143], %[f4], v[150:151] op_sel:[1,1,0] op_sel_hi:[1,0,1] neg_lo:[0,1,0]\n\t" \
    "v_pk_fma_f32 v[140:141], v[144:145], %[f8], v[152:153] op_sel:[1,1,0] op_sel_hi:[1,0,1] neg_lo:[0,1,0]\n\tv_cvt_pk_bf16_f32 v155, v142, v143\n\t" \
    "v_cvt_pk_bf16_f32 v156, v140, v141\n\tv_pk_mul_f32 v[150:151], %[g], v[140:141] op_sel_hi:[1,0]\n\t" \
    "v_pk_mul_f32 v[152:153], %[e8], v[146:147] op_sel_hi:[1,0]\n\tds_write_addtid_b32 v155 offset:27120\n\t" \
    "v_pk_fma_f32 v[140:141], v[140:141], %[g], v[150:151] op_sel:[1,1,0] op_sel_hi:[1,0,1] neg_lo:[0,1,0]\n\tv_pk_fma_f32 v[146:147], v[146:147], %[e8], v[152:153] op_sel:[1,1,0] op_sel_hi:[1,0,1] neg_lo:[0,1,0]\n\t" \
    "ds_write_addtid_b32 v156 offset:28928\n\tv_cvt_pk_bf16_f32 v157, v140, v141\n\t" \
    "v_pk_mul_f32 v[150:151], %[g], v[140:141] op_sel_hi:[1,0]\n\tds_write_addtid_b32 v157 offset:30736\n\t" \
    "v_pk_fma_f32 v[140:141], v[140:141], %[g], v[150:151] op_sel:[1,1,0] op_sel_hi:[1,0,1] neg_lo:[0,1,0]\n\ts_nop 0\n\t" \
    "v_cvt_pk_bf16_f32 v154, v140, v141\n\tv_pk_mul_f32 v[150:151], %[g], v[140:141] op_sel_hi:[1,0]\n\t" \
    "ds_write_addtid_b32 v154 offset:32544\n\tv_pk_fma_f32 v[140:141], v[140:141], %[g], v[150:151] op_sel:[1,1,0] op_sel_hi:[1,0,1] neg_lo:[0,1,0]\n\t" \
    "s_nop 0\n\tv_cvt_pk_bf16_f32 v155, v140, v141\n\t" \
    "ds_write_addtid_b32 v155 offset:34352\n\ts_mov_b32 m0, %[my]\n\t" \
    "v_cvt_pk_bf16_f32 v156, v146, v147\n\tv_cvt_pk_bf16_f32 v157, v148, v149\n\t" \
    "v_pk_mul_f32 v[150:151], %[e8], v[146:147] op_sel_hi:[1,0]\n\tv_pk_mul_f32 v[152:153], %[e8], v[148:149] op_sel_hi:[1,0]\n\t" \
    "ds_write_addtid_b32 v156 offset:1808\n\tv_pk_fma_f32 v[146:147], v[146:147], %[e8], v[150:151] op_sel:[1,1,0] op_sel_hi:[1,0,1] neg_lo:[0,1,0]\n\t" \
    "v_pk_fma_f32 v[148:149], v[148:149], %[e8], v[152:153] op_sel:[1,1,0] op_sel_hi:[1,0,1] neg_lo:[0,1,0]\n\tds_write_addtid_b32 v157 offset:28928\n\t" \
    "v_cvt_pk_bf16_f32 v154, v146, v147\n\tv_cvt_pk_bf16_f32 v155, v148, v149\n\t" \
    "v_pk_mul_f32 v[150:151], %[e8], v[146:147] op_sel_hi:[1,0]\n\tv_pk_mul_f32 v[152:153], %[e8], v[148:149] op_sel_hi:[1,0]\n\t" \
    "ds_write_addtid_b32 v154 offset:3616\n\tv_pk_fma_f32 v[146:147], v[146:147], %[e8], v[150:151] op_sel:[1,1,0] op_sel_hi:[1,0,1] neg_lo:[0,1,0]\n\t" \
    "v_pk_fma_f32 v[148:149], v[148:149], %[e8], v[152:153] op_sel:[1,1,0] op_sel_hi:[1,0,1] neg_lo:[0,1,0]\n\tds_write_addtid_b32 v155 offset:30736\n\t" \
    "v_cvt_pk_bf16_f32 v156, v146, v147\n\tv_cvt_pk_bf16_f32 v157, v148, v149\n\t" \
    "v_pk_mul_f32 v[150:151], %[e8], v[146:147] op_sel_hi:[1,0]\n\tv_pk_mul_f32 v[152:153], %[e8], v[148:149] op_sel_hi:[1,0]\n\t" \
    "ds_write_addtid_b32 v156 offset:5424\n\tv_pk_fma_f32 v[146:147], v[146:147], %[e8], v[150:151] op_sel:[1,1,0] op_sel_hi:[1,0,1] neg_lo:[0,1,0]\n\t" \
    "v_pk_fma_f32 v[148:149], v[148:149], %[e8], v[152:153] op_sel:[1,1,0] op_sel_hi:[1,0,1] neg_lo:[0,1,0]\n\tds_write_addtid_b32 v157 offset:32544\n\t" \
    "v_cvt_pk_bf16_f32 v154, v146, v147\n\tv_cvt_pk_bf16_f32 v155, v148, v149\n\t" \
    "v_pk_mul_f32 v[150:151], %[e8], v[146:147] op_sel_hi:[1,0]\n\tv_pk_mul_f32 v[152:153], %[e8], v[148:149] op_sel_hi:[1,0]\n\t" \
    "ds_write_addtid_b32 v154 offset:7232\n\tv_pk_fma_f32 v[146:147], v[146:147], %[e8], v[150:151] op_sel:[1,1,0] op_sel_hi:[1,0,1] neg_lo:[0,1,0]\n\t" \
    "v_pk_fma_f32 v[148:149], v[148:149], %[e8], v[152:153] op_sel:[1,1,0] op_sel_hi:[1,0,1] neg_lo:[0,1,0]\n\tds_write_addtid_b32 v155 offset:34352\n\t" \
    "v_cvt_pk_bf16_f32 v156, v146, v147\n\tv_cvt_pk_bf16_f32 v157, v148, v149\n\t" \
    "v_pk_mul_f32 v[150:151], %[e8], v[146:147] op_sel_hi:[1,0]\n\tv_pk_mul_f32 v[152:153], %[e8], v[148:149] op_sel_hi:[1,0]\n\t" \
    "ds_write_addtid_b32 v156 offset:9040\n\tv_pk_fma_f32 v[146:147], v[146:147], %[e8], v[150:151] op_sel:[1,1,0] op_sel_hi:[1,0,1] neg_lo:[0,1,0]\n\t" \
    "v_pk_fma_f32 v[148:149], v[148:149], %[e8], v[152:153] op_sel:[1,1,0] op_sel_hi:[1,0,1] neg_lo:[0,1,0]\n\tds_write_addtid_b32 v157 offset:36160\n\t" \
    "v_cvt_pk_bf16_f32 v154, v146, v147\n\tv_cvt_pk_bf16_f32 v155, v148, v149\n\t" \
    "v_pk_mul_f32 v[150:151], %[e8], v[146:147] op_sel_hi:[1,0]\n\tv_pk_mul_f32 v[152:153], %[e8], v[148:149] op_sel_hi:[1,0]\n\t" \
    "ds_write_addtid_b32 v154 offset:10848\n\tv_pk_fma_f32 v[146:147], v[146:147], %[e8], v[150:151] op_sel:[1,1,0] op_sel_hi:[1,0,1] neg_lo:[0,1,0]\n\t" \
    "v_pk_fma_f32 v[148:149], v[148:149], %[e8], v[152:153] op_sel:[1,1,0] op_sel_hi:[1,0,1] neg_lo:[0,1,0]\n\tds_write_addtid_b32 v155 offset:37968\n\t" \
    "v_cvt_pk_bf16_f32 v156, v146, v147\n\tv_cvt_pk_bf16_f32 v157, v148, v149\n\t" \
    "v_pk_mul_f32 v[150:151], %[e8], v[146:147] op_sel_hi:[1,0]\n\tv_pk_mul_f32 v[152:153], %[e8], v[148:149] op_sel_hi:[1,0]\n\t" \
    "ds_write_addtid_b32 v156 offset:12656\n\tv_pk_fma_f32 v[146:147], v[146:147], %[e8], v[150:151] op_sel:[1,1,0] op_sel_hi:[1,0,1] neg_lo:[0,1,0]\n\t" \
    "v_pk_fma_f32 v[148:149], v[148:149], %[e8], v[152:153] op_sel:[1,1,0] op_sel_hi:[1,0,1] neg_lo:[0,1,0]\n\tds_write_addtid_b32 v157 offset:39776\n\t" \
    "v_cvt_pk_bf16_f32 v154, v146, v147\n\tv_cvt_pk_bf16_f32 v155, v148, v149\n\t" \
    "v_pk_mul_f32 v[150:151], %[e8], v[146:147] op_sel_hi:[1,0]\n\tv_pk_mul_f32 v[152:153], %[e8], v[148:149] op_sel_hi:[1,0]\n\t" \
    "ds_write_addtid_b32 v154 offset:14464\n\tv_pk_fma_f32 v[146:147], v[146:147], %[e8], v[150:151] op_sel:[1,1,0] op_sel_hi:[1,0,1] neg_lo:[0,1,0]\n\t" \
    "v_pk_fma_f32 v[148:149], v[148:149], %[e8], v[152:153] op_sel:[1,1,0] op_sel_hi:[1,0,1] neg_lo:[0,1,0]\n\tds_write_addtid_b32 v155 offset:41584\n\t" \
    "v_cvt_pk_bf16_f32 v156, v146, v147\n\tv_cvt_pk_bf16_f32 v157, v148, v149\n\t" \
    "v_pk_mul_f32 v[150:151], %[e8], v[146:147] op_sel_hi:[1,0]\n\tv_pk_mul_f32 v[152:153], %[e8], v[148:149] op_sel_hi:[1,0]\n\t" \
    "ds_write_addtid_b32 v156 offset:16272\n\tv_pk_fma_f32 v[146:147], v[146:147], %[e8], v[150:151] op_sel:[1,1,0] op_sel_hi:[1,0,1] neg_lo:[0,1,0]\n\t" \
    "v_pk_fma_f32 v[148:149], v[148:149], %[e8], v[152:153] op_sel:[1,1,0] op_sel_hi:[1,0,1] neg_lo:[0,1,0]\n\tds_write_addtid_b32 v157 offset:43392\n\t" \
    "v_cvt_pk_bf16_f32 v154, v146, v147\n\tv_cvt_pk_bf16_f32 v155, v148, v149\n\t" \
    "v_pk_mul_f32 v[150:151], %[e8], v[146:147] op_sel_hi:[1,0]\n\tv_pk_mul_f32 v[152:153], %[e8], v[148:149] op_sel_hi:[1,0]\n\t" \
    "ds_write_addtid_b32 v154 offset:18080\n\tv_pk_fma_f32 v[146:147], v[146:147], %[e8], v[150:151] op_sel:[1,1,0] op_sel_hi:[1,0,1] neg_lo:[0,1,0]\n\t" \
    "v_pk_fma_f32 v[148:149], v[148:149], %[e8], v[152:153] op_sel:[1,1,0] op_sel_hi:[1,0,1] neg_lo:[0,1,0]\n\tds_write_addtid_b32 v155 offset:45200\n\t" \
    "v_cvt_pk_bf16_f32 v156, v146, v147\n\tv_cvt_pk_bf16_f32 v157, v148, v149\n\t" \
    "v_pk_mul_f32 v[150:151], %[e8], v[146:147] op_sel_hi:[1,0]\n\tv_pk_mul_f32 v[152:153], %[e8], v[148:149] op_sel_hi:[1,0]\n\t" \
    "ds_write_addtid_b32 v156 offset:19888\n\tv_pk_fma_f32 v[146:147], v[146:147], %[e8], v[150:151] op_sel:[1,1,0] op_sel_hi:[1,0,1] neg_lo:[0,1,0]\n\t" \
    "v_pk_fma_f32 v[148:149], v[148:149], %[e8], v[152:153] op_sel:[1,1,0] op_sel_hi:[1,0,1] neg_lo:[0,1,0]\n\tds_write_addtid_b32 v157 offset:47008\n\t" \
    "v_cvt_pk_bf16_f32 v154, v146, v147\n\tv_cvt_pk_bf16_f32 v155, v148, v149\n\t" \
    "v_pk_mul_f32 v[150:151], %[e8], v[146:147] op_sel_hi:[1,0]\n\tv_pk_mul_f32 v[152:153], %[e8], v[148:149] op_sel_hi:[1,0]\n\t" \
    "ds_write_addtid_b32 v154 offset:21696\n\tv_pk_fma_f32 v[146:147], v[146:147], %[e8], v[150:151] op_sel:[1,1,0] op_sel_hi:[1,0,1] neg_lo:[0,1,0]\n\t" \
    "v_pk_fma_f32 v[148:149], v[148:149], %[e8], v[152:153] op_sel:[1,1,0] op_sel_hi:[1,0,1] neg_lo:[0,1,0]\n\tds_write_addtid_b32 v155 offset:48816\n\t" \
    "v_cvt_pk_bf16_f32 v156, v146, v147\n\tv_cvt_pk_bf16_f32 v157, v148, v149\n\t" \
    "v_pk_mul_f32 v[150:151], %[e8], v[146:147] op_sel_hi:[1,0]\n\tv_pk_mul_f32 v[152:153], %[e8], v[148:149] op_sel_hi:[1,0]\n\t" \
    "ds_write_addtid_b32 v156 offset:23504\n\tv_pk_fma_f32 v[146:147], v[146:147], %[e8], v[150:151] op_sel:[1,1,0] op_sel_hi:[1,0,1] neg_lo:[0,1,0]\n\t" \
    "v_pk_fma_f32 v[148:149], v[148:149], %[e8], v[152:153] op_sel:[1,1,0] op_sel_hi:[1,0,1] neg_lo:[0,1,0]\n\tds_write_addtid_b32 v157 offset:50624\n\t" \
    "v_cvt_pk_bf16_f32 v154, v146, v147\n\tv_cvt_pk_bf16_f32 v155, v148, v149\n\t" \
    "v_pk_mul_f32 v[150:151], %[e8], v[146:147] op_sel_hi:[1,0]\n\tv_pk_mul_f32 v[152:153], %[e8], v[148:149] op_sel_hi:[1,0]\n\t" \
    "ds_write_addtid_b32 v154 offset:25312\n\tv_pk_fma_f32 v[146:147], v[146:147], %[e8], v[150:151] op_sel:[1,1,0] op_sel_hi:[1,0,1] neg_lo:[0,1,0]\n\t" \
    "v_pk_fma_f32 v[148:149], v[148:149], %[e8], v[152:153] op_sel:[1,1,0] op_sel_hi:[1,0,1] neg_lo:[0,1,0]\n\tds_write_addtid_b32 v155 offset:52432\n\t" \
    "v_cvt_pk_bf16_f32 v156, v146, v147\n\tv_cvt_pk_bf16_f32 v157, v148, v149\n\t" \
    "ds_write_addtid_b32 v156 offset:27120\n\tds_write_addtid_b32 v157 offset:54240\n\t"
#define KG_CHAINS_784 \
    "s_mov_b32 m0, %[mx]\n\tv_cvt_pk_bf16_f32 v154, v140, v141\n\t" \
    "v_cvt_pk_bf16_f32 v155, v146, v147\n\tv_mov_b32 v157, 0x3f80\n\t" \
    "ds_write_addtid_b32 v157 offset:16464\n\tv_pk_mul_f32 v[150:151], %[f1], v[140:141] op_sel_hi:[1,0]\n\t" \
    "v_pk_mul_f32 v[152:153], %[f2], v[142:143] op_sel_hi:[1,0]\n\tds_write_addtid_b32 v154 offset:15680\n\t" \
    "v_pk_fma_f32 v[140:141], v[140:141], %[f1], v[150:151] op_sel:[1,1,0] op_sel_hi:[1,0,1] neg_lo:[0,1,0]\n\tv_pk_fma_f32 v[142:143], v[142:143], %[f2], v[152:153] op_sel:[1,1,0] op_sel_hi:[1,0,1] neg_lo:[0,1,0]\n\t" \
    "ds_write_addtid_b32 v155 offset:17248\n\tv_cvt_pk_bf16_f32 v156, v140, v141\n\t" \
    "v_cvt_pk_bf16_f32 v157, v142, v143\n\tv_pk_mul_f32 v[150:151], %[f1], v[140:141] op_sel_hi:[1,0]\n\t" \
    "v_pk_mul_f32 v[152:153], %[f2], v[142:143] op_sel_hi:[1,0]\n\tds_write_addtid_b32 v156 offset:0\n\t" \
    "v_pk_fma_f32 v[140:141], v[140:141], %[f1], v[150:151] op_sel:[1,1,0] op_sel_hi:[1,0,1] neg_lo:[0,1,0]\n\tv_pk_fma_f32 v[142:143], v[142:143], %[f2], v[152:153] op_sel:[1,1,0] op_sel_hi:[1,0,1] neg_lo:[0,1,0]\n\t" \
    "ds_write_addtid_b32 v157 offset:6272\n\tv_cvt_pk_bf16_f32 v154, v140, v141\n\t" \
    "v_cvt_pk_bf16_f32 v155, v142, v143\n\tv_pk_mul_f32 v[150:151], %[f1], v[140:141] op_sel_hi:[1,0]\n\t" \
    "v_pk_mul_f32 v[152:153], %[f2], v[142:143] op_sel_hi:[1,0]\n\tds_write_addtid_b32 v154 offset:784\n\t" \
    "v_pk_fma_f32 v[140:141], v[140:141], %[f1], v[150:151] op_sel:[1,1,0] op_sel_hi:[1,0,1] neg_lo:[0,1,0]\n\tv_pk_fma_f32 v[142:143], v[142:143], %[f2], v[152:153] op_sel:[1,1,0] op_sel_hi:[1,0,1] neg_lo:[0,1,0]\n\t" \
    "ds_write_addtid_b32 v155 offset:7056\n\tv_cvt_pk_bf16_f32 v156, v140, v141\n\t" \
    "v_cvt_pk_bf16_f32 v157, v142, v143\n\tv_pk_mul_f32 v[150:151], %[f1], v[140:141] op_sel_hi:[1,0]\n\t" \
    "v_pk_mul_f32 v[152:153], %[f2], v[142:143] op_sel_hi:[1,0]\n\tds_write_addtid_b32 v156 offset:1568\n\t" \
    "v_pk_fma_f32 v[140:141], v[140:141], %[f1], v[150:151] op_sel:[1,1,0] op_sel_hi:[1,0,1] neg_lo:[0,1,0]\n\tv_pk_fma_f32 v[142:143], v[142:143], %[f2], v[152:153] op_sel:[1,1,0] op_sel_hi:[1,0,1] neg_lo:[0,1,0]\n\t" \
    "ds_write_addtid_b32 v157 offset:7840\n\tv_cvt_pk_bf16_f32 v154, v140, v141\n\t" \
    "v_cvt_pk_bf16_f32 v155, v142, v143\n\tv_pk_mul_f32 v[150:151], %[f1], v[140:141] op_sel_hi:[1,0]\n\t" \
    "v_pk_mul_f32 v[152:153], %[g], v[144:145] op_sel_hi:[1,0]\n\tds_write_addtid_b32 v154 offset:2352\n\t" \
    "v_pk_fma_f32 v[140:141], v[140:141], %[f1], v[150:151] op_sel:[1,1,0] op_sel_hi:[1,0,1] neg_lo:[0,1,0]\n\tv_pk_fma_f32 v[144:145], v[144:145], %[g], v[152:153] op_sel:[1,1,0] op_sel_hi:[1,0,1] neg_lo:[0,1,0]\n\t" \
    "ds_write_addtid_b32 v155 offset:8624\n\tv_cvt_pk_bf16_f32 v156, v140, v141\n\t" \
    "v_pk_mul_f32 v[150:151], %[f1], v[140:141] op_sel_hi:[1,0]\n\tv_pk_mul_f32 v[152:153], %[f4], v[144:145] op_sel_hi:[1,0]\n\t" \
    "ds_write_addtid_b32 v156 offset:3136\n\tv_pk_fma_f32 v[140:141], v[140:141], %[f1], v[150:151] op_sel:[1,1,0] op_sel_hi:[1,0,1] neg_lo:[0,1,0]\n\t" \
    "v_pk_fma_f32 v[142:143], v[144:145], %[f4], v[152:153] op_sel:[1,1,0] op_sel_hi:[1,0,1] neg_lo:[0,1,0]\n\tv_cvt_pk_bf16_f32 v157, v140, v141\n\t" \
    "v_cvt_pk_bf16_f32 v154, v142, v143\n\tv_pk_mul_f32 v[150:151], %[f1], v[140:141] op_sel_hi:[1,0]\n\t" \
    "v_pk_mul_f32 v[152:153], %[f4], v[142:143] op_sel_hi:[1,0]\n\tds_write_addtid_b32 v157 offset:3920\n\t" \
    "v_pk_fma_f32 v[140:141], v[140:141], %[f1], v[150:151] op_sel:[1,1,0] op_sel_hi:[1,0,1] neg_lo:[0,1,0]\n\tv_pk_fma_f32 v[142:143], v[142:143], %[f4], v[152:153] op_sel:[1,1,0] op_sel_hi:[1,0,1] neg_lo:[0,1,0]\n\t" \
    "ds_write_addtid_b32 v154 offset:9408\n\tv_cvt_pk_bf16_f32 v155, v140, v141\n\t" \
    "v_cvt_pk_bf16_f32 v156, v142, v143\n\tv_pk_mul_f32 v[150:151], %[f1], v[140:141] op_sel_hi:[1,0]\n\t" \
    "v_pk_mul_f32 v[152:153], %[g], v[144:145] op_sel_hi:[1,0]\n\tds_write_addtid_b32 v155 offset:4704\n\t" \
    "v_pk_fma_f32 v[140:141], v[140:141], %[f1], v[150:151] op_sel:[1,1,0] op_sel_hi:[1,0,1] neg_lo:[0,1,0]\n\tv_pk_fma_f32 v[144:145], v[144:145], %[g], v[152:153] op_sel:[1,1,0] op_sel_hi:[1,0,1] neg_lo:[0,1,0]\n\t" \
    "ds_write_addtid_b32 v156 offset:10192\n\tv_cvt_pk_bf16_f32 v157, v140, v141\n\t" \
    "v_pk_mul_f32 v[150:151], %[f4], v[144:145] op_sel_hi:[1,0]\n\tv_pk_mul_f32 v[152:153], %[g], v[144:145] op_sel_hi:[1,0]\n\t" \
    "ds_write_addtid_b32 v157 offset:5488\n\tv_pk_fma_f32 v[142:143], v[144:145], %[f4], v[150:151] op_sel:[1,1,0] op_sel_hi:[1,0,1] neg_lo:[0,1,0]\n\t" \
    "v_pk_fma_f32 v[144:145], v[144:145], %[g], v[152:153] op_sel:[1,1,0] op_sel_hi:[1,0,1] neg_lo:[0,1,0]\n\tv_cvt_pk_bf16_f32 v154, v142, v143\n\t" \
    "v_pk_mul_f32 v[150:151], %[f4], v[142:143] op_sel_hi:[1,0]\n\tv_pk_mul_f32 v[152:153], %[f8], v[144:145] op_sel_hi:[1,0]\n\t" \
    "ds_write_addtid_b32 v154 offset:10976\n\tv_pk_fma_f32 v[142:143], v[142:143], %[f4], v[150:151] op_sel:[1,1,0] op_sel_hi:[1,0,1] neg_lo:[0,1,0]\n\t" \
    "v_pk_fma_f32 v[140:141], v[144:145], %[f8], v[152:153] op_sel:[1,1,0] op_sel_hi:[1,0,1] neg_lo:[0,1,0]\n\tv_cvt_pk_bf16_f32 v155, v142, v143\n\t" \
    "v_cvt_pk_bf16_f32 v156, v140, v141\n\tv_pk_mul_f32 v[150:151], %[g], v[140:141] op_sel_hi:[1,0]\n\t" \
    "v_pk_mul_f32 v[152:153], %[e8], v[146:147] op_sel_hi:[1,0]\n\tds_write_addtid_b32 v155 offset:11760\n\t" \
    "v_pk_fma_f32 v[140:141], v[140:141], %[g], v[150:151] op_sel:[1,1,0] op_sel_hi:[1,0,1] neg_lo:[0,1,0]\n\tv_pk_fma_f32 v[146:147], v[146:147], %[e8], v[152:153] op_sel:[1,1,0] op_sel_hi:[1,0,1] neg_lo:[0,1,0]\n\t" \
    "ds_write_addtid_b32 v156 offset:12544\n\tv_cvt_pk_bf16_f32 v157, v140, v141\n\t" \
    "v_pk_mul_f32 v[150:151], %[g], v[140:141] op_sel_hi:[1,0]\n\tds_write_addtid_b32 v157 offset:13328\n\t" \
    "v_pk_fma_f32 v[140:141], v[140:141], %[g], v[150:151] op_sel:[1,1,0] op_sel_hi:[1,0,1] neg_lo:[0,1,0]\n\ts_nop 0\n\t" \
    "v_cvt_pk_bf16_f32 v154, v140, v141\n\tv_pk_mul_f32 v[150:151], %[g], v[140:141] op_sel_hi:[1,0]\n\t" \
    "ds_write_addtid_b32 v154 offset:14112\n\tv_pk_fma_f32 v[140:141], v[140:141], %[g], v[150:151] op_sel:[1,1,0] op_sel_hi:[1,0,1] neg_lo:[0,1,0]\n\t" \
    "s_nop 0\n\tv_cvt_pk_bf16_f32 v155, v140, v141\n\t" \
    "ds_write_addtid_b32 v155 offset:14896\n\ts_mov_b32 m0, %[my]\n\t" \
    "v_cvt_pk_bf16_f32 v156, v146, v147\n\tv_cvt_pk_bf16_f32 v157, v148, v149\n\t" \
    "v_pk_mul_f32 v[150:151], %[e8], v[146:147] op_sel_hi:[1,0]\n\tv_pk_mul_f32 v[152:153], %[e8], v[148:149] op_sel_hi:[1,0]\n\t" \
    "ds_write_addtid_b32 v156 offset:784\n\tv_pk_fma_f32 v[146:147], v[146:147], %[e8], v[150:151] op_sel:[1,1,0] op_sel_hi:[1,0,1] neg_lo:[0,1,0]\n\t" \
    "v_pk_fma_f32 v[148:149], v[148:149], %[e8], v[152:153] op_sel:[1,1,0] op_sel_hi:[1,0,1] neg_lo:[0,1,0]\n\tds_write_addtid_b32 v157 offset:12544\n\t" \
    "v_cvt_pk_bf16_f32 v154, v146, v147\n\tv_cvt_pk_bf16_f32 v155, v148, v149\n\t" \
    "v_pk_mul_f32 v[150:151], %[e8], v[146:147] op_sel_hi:[1,0]\n\tv_pk_mul_f32 v[152:153], %[e8], v[148:149] op_sel_hi:[1,0]\n\t" \
    "ds_write_addtid_b32 v154 offset:1568\n\tv_pk_fma_f32 v[146:147], v[146:147], %[e8], v[150:151] op_sel:[1,1,0] op_sel_hi:[1,0,1] neg_lo:[0,1,0]\n\t" \
    "v_pk_fma_f32 v[148:149], v[148:149], %[e8], v[152:153] op_sel:[1,1,0] op_sel_hi:[1,0,1] neg_lo:[0,1,0]\n\tds_write_addtid_b32 v155 offset:13328\n\t" \
    "v_cvt_pk_bf16_f32 v156, v146, v147\n\tv_cvt_pk_bf16_f32 v157, v148, v149\n\t" \
    "v_pk_mul_f32 v[150:151], %[e8], v[146:147] op_sel_hi:[1,0]\n\tv_pk_mul_f32 v[152:153], %[e8], v[148:149] op_sel_hi:[1,0]\n\t" \
    "ds_write_addtid_b32 v156 offset:2352\n\tv_pk_fma_f32 v[146:147], v[146:147], %[e8], v[150:151] op_sel:[1,1,0] op_sel_hi:[1,0,1] neg_lo:[0,1,0]\n\t" \
    "v_pk_fma_f32 v[148:149], v[148:149], %[e8], v[152:153] op_sel:[1,1,0] op_sel_hi:[1,0,1] neg_lo:[0,1,0]\n\tds_write_addtid_b32 v157 offset:14112\n\t" \
    "v_cvt_pk_bf16_f32 v154, v146, v147\n\tv_cvt_pk_bf16_f32 v155, v148, v149\n\t" \
    "v_pk_mul_f32 v[150:151], %[e8], v[146:147] op_sel_hi:[1,0]\n\tv_pk_mul_f32 v[152:153], %[e8], v[148:149] op_sel_hi:[1,0]\n\t" \
    "ds_write_addtid_b32 v154 offset:3136\n\tv_pk_fma_f32 v[146:147], v[146:147], %[e8], v[150:151] op_sel:[1,1,0] op_sel_hi:[1,0,1] neg_lo:[0,1,0]\n\t" \
    "v_pk_fma_f32 v[148:149], v[148:149], %[e8], v[152:153] op_sel:[1,1,0] op_sel_hi:[1,0,1] neg_lo:[0,1,0]\n\tds_write_addtid_b32 v155 offset:14896\n\t" \
    "v_cvt_pk_bf16_f32 v156, v146, v147\n\tv_cvt_pk_bf16_f32 v157, v148, v149\n\t" \
    "v_pk_mul_f32 v[150:151], %[e8], v[146:147] op_sel_hi:[1,0]\n\tv_pk_mul_f32 v[152:153], %[e8], v[148:149] op_sel_hi:[1,0]\n\t" \
    "ds_write_addtid_b32 v156 offset:3920\n\tv_pk_fma_f32 v[146:147], v[146:147], %[e8], v[150:151] op_sel:[1,1,0] op_sel_hi:[1,0,1] neg_lo:[0,1,0]\n\t" \
    "v_pk_fma_f32 v[148:149], v[148:149], %[e8], v[152:153] op_sel:[1,1,0] op_sel_hi:[1,0,1] neg_lo:[0,1,0]\n\tds_write_addtid_b32 v157 offset:15680\n\t" \
    "v_cvt_pk_bf16_f32 v154, v146, v147\n\tv_cvt_pk_bf16_f32 v155, v148, v149\n\t" \
    "v_pk_mul_f32 v[150:151], %[e8], v[146:147] op_sel_hi:[1,0]\n\tv_pk_mul_f32 v[152:153], %[e8], v[148:149] op_sel_hi:[1,0]\n\t" \
    "ds_write_addtid_b32 v154 offset:4704\n\tv_pk_fma_f32 v[146:147], v[146:147], %[e8], v[150:151] op_sel:[1,1,0] op_sel_hi:[1,0,1] neg_lo:[0,1,0]\n\t" \
    "v_pk_fma_f32 v[148:149], v[148:149], %[e8], v[152:153] op_sel:[1,1,0] op_sel_hi:[1,0,1] neg_lo:[0,1,0]\n\tds_write_addtid_b32 v155 offset:16464\n\t" \
    "v_cvt_pk_bf16_f32 v156, v146, v147\n\tv_cvt_pk_bf16_f32 v157, v148, v149\n\t" \
    "v_pk_mul_f32 v[150:151], %[e8], v[146:147] op_sel_hi:[1,0]\n\tv_pk_mul_f32 v[152:153], %[e8], v[148:149] op_sel_hi:[1,0]\n\t" \
    "ds_write_addtid_b32 v156 offset:5488\n\tv_pk_fma_f32 v[146:147], v[146:147], %[e8], v[150:151] op_sel:[1,1,0] op_sel_hi:[1,0,1] neg_lo:[0,1,0]\n\t" \
    "v_pk_fma_f32 v[148:149], v[148:149], %[e8], v[152:153] op_sel:[1,1,0] op_sel_hi:[1,0,1] neg_lo:[0,1,0]\n\tds_write_addtid_b32 v157 offset:17248\n\t" \
    "v_cvt_pk_bf16_f32 v154, v146, v147\n\tv_cvt_pk_bf16_f32 v155, v148, v149\n\t" \
    "v_pk_mul_f32 v[150:151], %[e8], v[146:147] op_sel_hi:[1,0]\n\tv_pk_mul_f32 v[152:153], %[e8], v[148:149] op_sel_hi:[1,0]\n\t" \
    "ds_write_addtid_b32 v154 offset:6272\n\tv_pk_fma_f32 v[146:147], v[146:147], %[e8], v[150:151] op_sel:[1,1,0] op_sel_hi:[1,0,1] neg_lo:[0,1,0]\n\t" \
    "v_pk_fma_f32 v[148:149], v[148:149], %[e8], v[152:153] op_sel:[1,1,0] op_sel_hi:[1,0,1] neg_lo:[0,1,0]\n\tds_write_addtid_b32 v155 offset:18032\n\t" \
    "v_cvt_pk_bf16_f32 v156, v146, v147\n\tv_cvt_pk_bf16_f32 v157, v148, v149\n\t" \
    "v_pk_mul_f32 v[150:151], %[e8], v[146:147] op_sel_hi:[1,0]\n\tv_pk_mul_f32 v[152:153], %[e8], v[148:149] op_sel_hi:[1,0]\n\t" \
    "ds_write_addtid_b32 v156 offset:7056\n\tv_pk_fma_f32 v[146:147], v[146:147], %[e8], v[150:151] op_sel:[1,1,0] op_sel_hi:[1,0,1] neg_lo:[0,1,0]\n\t" \
    "v_pk_fma_f32 v[148:149], v[148:149], %[e8], v[152:153] op_sel:[1,1,0] op_sel_hi:[1,0,1] neg_lo:[0,1,0]\n\tds_write_addtid_b32 v157 offset:18816\n\t" \
    "v_cvt_pk_bf16_f32 v154, v146, v147\n\tv_cvt_pk_bf16_f32 v155, v148, v149\n\t" \
    "v_pk_mul_f32 v[150:151], %[e8], v[146:147] op_sel_hi:[1,0]\n\tv_pk_mul_f32 v[152:153], %[e8], v[148:149] op_sel_hi:[1,0]\n\t" \
    "ds_write_addtid_b32 v154 offset:7840\n\tv_pk_fma_f32 v[146:147], v[146:147], %[e8], v[150:151] op_sel:[1,1,0] op_sel_hi:[1,0,1] neg_lo:[0,1,0]\n\t" \
    "v_pk_fma_f32 v[148:149], v[148:149], %[e8], v[152:153] op_sel:[1,1,0] op_sel_hi:[1,0,1] neg_lo:[0,1,0]\n\tds_write_addtid_b32 v155 offset:19600\n\t" \
    "v_cvt_pk_bf16_f32 v156, v146, v147\n\tv_cvt_pk_bf16_f32 v157, v148, v149\n\t" \
    "v_pk_mul_f32 v[150:151], %[e8], v[146:147] op_sel_hi:[1,0]\n\tv_pk_mul_f32 v[152:153], %[e8], v[148:149] op_sel_hi:[1,0]\n\t" \
    "ds_write_addtid_b32 v156 offset:8624\n\tv_pk_fma_f32 v[146:147], v[146:147], %[e8], v[150:151] op_sel:[1,1,0] op_sel_hi:[1,0,1] neg_lo:[0,1,0]\n\t" \
    "v_pk_fma_f32 v[148:149], v[148:149], %[e8], v[152:153] op_sel:[1,1,0] op_sel_hi:[1,0,1] neg_lo:[0,1,0]\n\tds_write_addtid_b32 v157 offset:20384\n\t" \
    "v_cvt_pk_bf16_f32 v154, v146, v147\n\tv_cvt_pk_bf16_f32 v155, v148, v149\n\t" \
    "v_pk_mul_f32 v[150:151], %[e8], v[146:147] op_sel_hi:[1,0]\n\tv_pk_mul_f32 v[152:153], %[e8], v[148:149] op_sel_hi:[1,0]\n\t" \
    "ds_write_addtid_b32 v154 offset:9408\n\tv_pk_fma_f32 v[146:147], v[146:147], %[e8], v[150:151] op_sel:[1,1,0] op_sel_hi:[1,0,1] neg_lo:[0,1,0]\n\t" \
    "v_pk_fma_f32 v[148:149], v[148:149], %[e8], v[152:153] op_sel:[1,1,0] op_sel_hi:[1,0,1] neg_lo:[0,1,0]\n\tds_write_addtid_b32 v155 offset:21168\n\t" \
    "v_cvt_pk_bf16_f32 v156, v146, v147\n\tv_cvt_pk_bf16_f32 v157, v148, v149\n\t" \
    "v_pk_mul_f32 v[150:151], %[e8], v[146:147] op_sel_hi:[1,0]\n\tv_pk_mul_f32 v[152:153], %[e8], v[148:149] op_sel_hi:[1,0]\n\t" \
    "ds_write_addtid_b32 v156 offset:10192\n\tv_pk_fma_f32 v[146:147], v[146:147], %[e8], v[150:151] op_sel:[1,1,0] op_sel_hi:[1,0,1] neg_lo:[0,1,0]\n\t" \
    "v_pk_fma_f32 v[148:149], v[148:149], %[e8], v[152:153] op_sel:[1,1,0] op_sel_hi:[1,0,1] neg_lo:[0,1,0]\n\tds_write_addtid_b32 v157 offset:21952\n\t" \
    "v_cvt_pk_bf16_f32 v154, v146, v147\n\tv_cvt_pk_bf16_f32 v155, v148, v149\n\t" \
    "v_pk_mul_f32 v[150:151], %[e8], v[146:147] op_sel_hi:[1,0]\n\tv_pk_mul_f32 v[152:153], %[e8], v[148:149] op_sel_hi:[1,0]\n\t" \
    "ds_write_addtid_b32 v154 offset:10976\n\tv_pk_fma_f32 v[146:147], v[146:147], %[e8], v[150:151] op_sel:[1,1,0] op_sel_hi:[1,0,1] neg_lo:[0,1,0]\n\t" \
    "v_pk_fma_f32 v[148:149], v[148:149], %[e8], v[152:153] op_sel:[1,1,0] op_sel_hi:[1,0,1] neg_lo:[0,1,0]\n\tds_write_addtid_b32 v155 offset:22736\n\t" \
    "v_cvt_pk_bf16_f32 v156, v146, v147\n\tv_cvt_pk_bf16_f32 v157, v148, v149\n\t" \
    "ds_write_addtid_b32 v156 offset:11760\n\tds_write_addtid_b32 v157 offset:23520\n\t"

struct KgS { cf T, U, WG, Y, Y2, F1, F2, F4, F8, G, E8; };
DI KgS kg_setup(const KgP& P, float dt) {
    const float zr = dt * bf2f(P.are), zi = dt * bf2f(P.aim);
    const float br = dt * bf2f(P.bre), bi = dt * bf2f(P.bim);
    const float cr = bf2f(P.cre), ci = bf2f(P.cim);
    const cf w = {cr * br - ci * bi, cr * bi + ci * br};
    const float zl = zr * LOG2E, zv = zi * 0.15915494309189535f;
    KgS s;
    s.F1 = cexp_(zl, zv, 1.f);
    const cf F128 = cexp_(zl, zv, 128.f);
    s.F2 = csq(s.F1); s.F4 = csq(s.F2); s.F8 = csq(s.F4); s.G = csq(F128);
    s.E8 = (cf){s.F8[0], -s.F8[1]};
    s.T = w; s.U = cmul(w, s.G); s.WG = s.U; s.Y = s.E8; s.Y2 = cmul((cf){F128[0], -F128[1]}, s.E8);
    return s;
}
#define KG_ASM(TEXT, S, TAB, ROW) asm volatile(TEXT \
        : "+&{v[140:141]}"(S.T), "+&{v[142:143]}"(S.U), "+&{v[144:145]}"(S.WG), "+&{v[146:147]}"(S.Y), "+&{v[148:149]}"(S.Y2) \
        : [f1] "v"(S.F1), [f2] "v"(S.F2), [f4] "v"(S.F4), [f8] "v"(S.F8), [g] "v"(S.G), [e8] "v"(S.E8), [mx] "s"(TAB), [my] "s"((TAB) + 22 * (ROW)) \
        : "v150", "v151", "v152", "v153", "v154", "v155", "v156", "v157", "memory")
template <int ROW>
DI void kg_products(LAS char* lds, int tab, int lane, f32x16& acc) {
    asm volatile("s_waitcnt lgkmcnt(0)" ::: "memory");
    const LAS char* ra = lds + tab + (lane & 31) * ROW + (lane >> 5) * 16;
#pragma unroll
    for (int ks = 0; ks < 8; ++ks) {
        bf16x8 a = *(const LAS bf16x8*)(ra + ks * 32);
        bf16x8 b = *(const LAS bf16x8*)(ra + 21 * ROW + ks * 32);
        acc = mfma32(a, b, acc);
    }
    WAIT_LGKM0();
}

DI void kg_finish(const f32x16& acc, int ch, rsrc_t rws, LAS char* kc, int lane) {
    const int b = lane & 31, h = lane >> 5;
    *(LAS u32x2*)(kc + (636 - 8 * b - 4 * h) * 2) = (u32x2){pk2(acc[3], acc[2]), pk2(acc[1], acc[0])};
    *(LAS unsigned*)(kc + (h ? 254 - 2 * b : 382 - 4 * b) * 2) = pk2(acc[5], acc[4]);
    *(LAS unsigned*)(kc + (h ? 190 - 2 * b : 380 - 4 * b) * 2) = pk2(acc[7], acc[6]);
    const unsigned p0 = pk2(acc[8], acc[9]), p1 = pk2(acc[10], acc[11]);
    if (h == 0) {
        *(LAS u16*)(kc + (127 - b) * 2) = (u16)p0;
        *(LAS u16*)(kc + (95 - b) * 2) = (u16)(p0 >> 16);
        *(LAS u16*)(kc + (63 - b) * 2) = (u16)p1;
        *(LAS u16*)(kc + (31 - b) * 2) = (u16)(p1 >> 16);
    } else if (b < 8) {
        *(LAS unsigned*)(kc + (640 + 2 * b) * 2) = b == 0 ? (p0 & 0xffffu) : 0u;
    }
    WAIT_LGKM0();
    const unsigned dst = (unsigned)WS_CH + ch * CHREC + 4096 + lane * 16;
    const u32x4 c0 = *(const LAS u32x4*)(kc + lane * 16);
    const u32x4 c1 = *(const LAS u32x4*)(kc + 1024 + (lane < 18 ? lane : 0) * 16);
    st16w(rws, dst, c0);
    if (lane < 18) st16w(rws, dst + 1024, c1);
}

DI void lds_signal(LAS char* f) { WAIT_LGKM0(); *(volatile LAS unsigned*)f = 1u; }
DI void lds_wait(LAS char* f) {
    for (int spin = 0; spin < (1 << 20); ++spin) {
        if (__builtin_amdgcn_readfirstlane(*(volatile LAS unsigned*)f)) break;
        __builtin_amdgcn_s_sleep(1);
    }
    asm volatile("" ::: "memory");
}

constexpr int K1_STAGE = 36864;
constexpr int KL_TAB = 0;
constexpr int KL_KC = 53 * 784;
constexpr int KL_FLAG = KL_KC + 1312 + 64;
constexpr int K1_RING = 43008;
constexpr int K1_LDS = K1_RING + 3 * K1_STAGE;
static_assert(KL_FLAG + 32 <= K1_RING, "lds");
constexpr int NCH_KG = 448;

template <bool ISQ>
DI void k1_gemm(const u16* __restrict__ x, const u16* __restrict__ wqkv, const KgIn& kin, char* __restrict__ ws, LAS char* lds,
                int rb, int cgi, int rot, int gi, int wave, int lane) {
    const int g = lane >> 4, l15 = lane & 15;
    const rsrc_t rws = ws_rsrc(ws);
    if (wave >= 4) {
        const int lw = wave - 4;
        __amdgpu_buffer_rsrc_t rx = __builtin_amdgcn_make_buffer_rsrc((void*)x, 0, L * D * 2, 0x00020000);
        __amdgpu_buffer_rsrc_t rw = __builtin_amdgcn_make_buffer_rsrc((void*)wqkv, 0, D * 3 * D * 2, 0x00020000);
        const int xo0 = (lane >> 3) * (D * 2) + (((lane & 7) ^ (lane >> 4)) * 16), xo1 = xo0 ^ 64;
        const int sub = (lane & 3) ^ ((lane >> 5) << 1);
        const int wo0 = (lane >> 2) * (3 * D * 2) + sub * 16;
        const int wo1 = ISQ ? wo0 : (lane >> 2) * (3 * D * 2) + (sub >> 1) * (D * 2) + (sub & 1) * 16;
        const int odd = cgi & 1;
        const int xs0 = (128 * rb + 32 * lw) * (D * 2);
        const int wsb = 16 * lw * (3 * D * 2) + (ISQ ? 320 * cgi : D * 2 + 160 * cgi);
        auto issue = [&](int t, int slot) {
            LAS char* buf = lds + K1_RING + slot * K1_STAGE;
            int s = t + rot; s = s >= 10 ? s - 10 : s;
#pragma unroll
            for (int i = 0; i < 4; ++i)
                __builtin_amdgcn_raw_ptr_buffer_load_lds(rx, (LAS void*)(buf + (4 * lw + i) * 1024), 16, (i & 1) ? xo1 : xo0,
                                                         xs0 + 8 * i * (D * 2) + s * 128, 0, 0);
#pragma unroll
            for (int i = 0; i < 5; ++i) {
                const int co = ISQ ? 64 * i : (i < 4 ? (i >> 1) * (D * 2) + (i & 1) * 64 + 32 * odd : (odd ? 0 : 128));
                __builtin_amdgcn_raw_ptr_buffer_load_lds(rw, (LAS void*)(buf + 16384 + i * 4096 + lw * 1024), 16, i < 4 ? wo0 : wo1,
                                                         wsb + co + s * (64 * 3 * D * 2), 0, 0);
            }
        };
        issue(0, 0); issue(1, 1);
        if (lw == 0 && lane < 8) *(LAS unsigned*)(lds + KL_FLAG + lane * 4) = 0u;
        WAIT_VM(9); WAIT_LGKM0(); BAR(); issue(2, 2);
        {
            int slot = 0;
#pragma unroll 1
            for (int t = 1; t < 8; ++t) { WAIT_VM(9); BAR(); issue(t + 2, slot); slot = slot == 2 ? 0 : slot + 1; }
        }
        WAIT_VM(9); BAR();
        WAIT_VM(0); BAR();
        if (lw == 0) {
            f32x16 acc;
            const LAS char* tb = lds + KL_TAB + lane * 4;
#pragma unroll
            for (int i = 0; i < 12; ++i) acc[i] = *(const LAS float*)(tb + i * 784) + *(const LAS float*)(tb + 256 + i * 784) + *(const LAS float*)(tb + 512 + i * 784);
            kg_finish(acc, NCH_KG + gi, rws, lds + KL_KC, lane);
        }
        return;
    }
    if (wave < 3) {
        const int kch = NCH_KG + gi;
        const KgP P = kg_load(kin, kch, wave, lane);
        const float dt = kg_dt(kin.logdt[kch]);
        KgS S = kg_setup(P, dt);
        const int tab = KL_TAB + wave * 256;
        KG_ASM(KG_CHAINS_784, S, tab, 784);
        f32x16 ka;
#pragma unroll
        for (int i = 0; i < 16; ++i) ka[i] = 0.f;
        kg_products<784>(lds, tab, lane, ka);
        LAS char* tb = lds + tab + lane * 4;
#pragma unroll
        for (int i = 0; i < 12; ++i) *(LAS float*)(tb + i * 784) = ka[i];
        WAIT_LGKM0();
    }
    const int wr = wave >> 1, wc = wave & 1;
    int xa[2];
#pragma unroll
    for (int ks = 0; ks < 2; ++ks) xa[ks] = (64 * wr + l15) * 128 + (((4 * ks + g) ^ (l15 >> 1)) * 16);
    int wb[5];
    {
        const int q = l15 >> 2, p = lane & 3;
#pragma unroll
        for (int c = 0; c < 5; ++c) {
            int pos;
            if (ISQ) pos = 2 * (5 * wc + c) + (p >> 1);
            else {
                const int i = 2 * c + (p >> 1);
                const int isv = i >= 5, kc = 5 * wc + (isv ? i - 5 : i), odd = cgi & 1;
                const int in_rest = odd ? (kc < 2) : (kc >= 8);
                pos = in_rest ? 16 + 2 * isv + (kc & 1) : 8 * isv + kc - 2 * odd;
            }
            wb[c] = 16384 + (pos >> 2) * 4096 + (8 * g + q) * 64 + ((pos & 3) ^ ((g & 1) << 1)) * 16 + (p & 1) * 8;
        }
    }
    f32x4 acc[4][5];
#pragma unroll
    for (int m = 0; m < 4; ++m)
#pragma unroll
        for (int c = 0; c < 5; ++c) acc[m][c] = (f32x4){0.f, 0.f, 0.f, 0.f};
    bf16x8 fx[2][4], fw[2][5];
    auto rd = [&](int b, int boff, int ks) {
        LAS char* bx = lds + boff + xa[ks];
        fx[b][0] = *(const LAS bf16x8*)(bx);
#pragma unroll
        for (int c = 0; c < 5; ++c) {
            LAS char* bw = lds + boff + wb[c] + ks * (32 * 64);
            fw[b][c] = lds_frag_tr(bw, bw + 4 * 64);
        }
#pragma unroll
        for (int m = 1; m < 4; ++m) fx[b][m] = *(const LAS bf16x8*)(bx + m * 2048);
    };
    auto mm = [&](int b) {
#pragma unroll
        for (int m = 0; m < 4; ++m)
#pragma unroll
            for (int c = 0; c < 5; ++c)
                acc[m][c] = ISQ ? mfma16(fw[b][c], fx[b][m], acc[m][c]) : mfma16(fx[b][m], fw[b][c], acc[m][c]);
    };
    auto interleave = [&]() {
#pragma unroll
        for (int i = 0; i < 14; ++i) {
            __builtin_amdgcn_sched_group_barrier(0x008, 1, 0);
            __builtin_amdgcn_sched_group_barrier(0x100, 1, 0);
        }
        __builtin_amdgcn_sched_group_barrier(0x008, 6, 0);
    };
#pragma unroll
    for (int m = 0; m < 4; ++m)
#pragma unroll
        for (int c = 0; c < 5; ++c) asm volatile("" : "+v"(acc[m][c]));
    BAR();
    rd(0, K1_RING, 0);
    int boff = K1_RING;
#pragma unroll 1
    for (int s = 0; s < 9; ++s) {
        __builtin_amdgcn_sched_barrier(0);
        rd(1, boff, 1);
        mm(0);
        interleave();
        __builtin_amdgcn_sched_barrier(0);
        boff = boff == K1_RING + 2 * K1_STAGE ? K1_RING : boff + K1_STAGE;
        WAIT_LGKM0(); BAR();
        __builtin_amdgcn_sched_barrier(0);
        rd(0, boff, 0);
        mm(1);
        interleave();
    }
    __builtin_amdgcn_sched_barrier(0);
    rd(1, boff, 1);
    mm(0);
    interleave();
    __builtin_amdgcn_sched_barrier(0);
    mm(1);
    __builtin_amdgcn_sched_barrier(0);
    if (ISQ) {
#pragma unroll
        for (int m = 0; m < 4; ++m) {
            const int rt = 8 * rb + 4 * wr + m;
            u32x2 h[5];
#pragma unroll
            for (int c = 0; c < 5; ++c) {
                h[c][0] = pk2f(phi2(lo2(acc[m][c])));
                h[c][1] = pk2f(phi2(hi2(acc[m][c])));
            }
            const unsigned base = (unsigned)WS_PQF + ((rt * 20 + 5 * cgi) * 64 + lane) * 16;
            if (wc == 0) {
                st16w(rws, base, (u32x4){h[0][0], h[0][1], h[1][0], h[1][1]});
                st16w(rws, base + 1024, (u32x4){h[2][0], h[2][1], h[3][0], h[3][1]});
                st8w(rws, base + 2048, h[4]);
            } else {
                st8w(rws, base + 2048 + 8, h[0]);
                st16w(rws, base + 3072, (u32x4){h[1][0], h[1][1], h[2][0], h[2][1]});
                st16w(rws, base + 4096, (u32x4){h[3][0], h[3][1], h[4][0], h[4][1]});
            }
        }
    } else {
        u32x4 F[5][2];
        const bool isk2 = l15 < 8;
#pragma unroll
        for (int mp = 0; mp < 2; ++mp)
#pragma unroll
            for (int c = 0; c < 5; ++c) {
#pragma unroll
                for (int j = 0; j < 4; ++j) {
                    const f32x2 a = (j & 1) ? hi2(acc[2 * mp + (j >> 1)][c]) : lo2(acc[2 * mp + (j >> 1)][c]);
                    f32x2 v = a;
                    if (c <= 2) {
                        const f32x2 ph = phi2(a);
                        v = c < 2 ? ph : (f32x2){isk2 ? ph[0] : a[0], isk2 ? ph[1] : a[1]};
                    }
                    F[c][mp][j] = pk2f(v);
                }
            }
        u32x4 T2o[2];
#pragma unroll
        for (int mp = 0; mp < 2; ++mp)
#pragma unroll
            for (int j = 0; j < 4; ++j) T2o[mp][j] = isk2 ? 0x3f803f80u : F[2][mp][j];
        f32x4 Dt[8];
#pragma unroll
        for (int i = 0; i < 8; ++i) Dt[i] = (f32x4){0.f, 0.f, 0.f, 0.f};
#pragma unroll
        for (int mp = 0; mp < 2; ++mp) {
            Dt[0] = mfma16(asfrag(T2o[mp]), asfrag(F[0][mp]), Dt[0]);
            Dt[1] = mfma16(asfrag(F[3][mp]), asfrag(F[0][mp]), Dt[1]);
            Dt[2] = mfma16(asfrag(T2o[mp]), asfrag(F[1][mp]), Dt[2]);
            Dt[3] = mfma16(asfrag(F[3][mp]), asfrag(F[1][mp]), Dt[3]);
            Dt[4] = mfma16(asfrag(F[4][mp]), asfrag(F[1][mp]), Dt[4]);
            Dt[5] = mfma16(asfrag(F[3][mp]), asfrag(F[2][mp]), Dt[5]);
            Dt[6] = mfma16(asfrag(F[4][mp]), asfrag(F[2][mp]), Dt[6]);
            Dt[7] = mfma16(asfrag(T2o[mp]), asfrag(F[2][mp]), Dt[7]);
        }
        LAS char* red = lds + K1_RING + 1 * K1_STAGE;
        LAS f32x4* mine = (LAS f32x4*)(red + wave * 4096) + lane;
        LAS f32x4* other = (LAS f32x4*)(red + (wave ^ 2) * 4096) + lane;
        f32x4 R[4];
        if (wr == 0) {
#pragma unroll
            for (int i = 0; i < 4; ++i) { mine[i * 64] = Dt[4 + i]; R[i] = Dt[i]; }
        } else {
#pragma unroll
            for (int i = 0; i < 4; ++i) { mine[i * 64] = Dt[i]; R[i] = Dt[4 + i]; }
        }
        lds_signal(lds + KL_FLAG + (2 + wave) * 4);
        lds_wait(lds + KL_FLAG + (2 + (wave ^ 2)) * 4);
#pragma unroll
        for (int i = 0; i < 4; ++i) R[i] += other[i * 64];
        const int hp = 2 * cgi + wc;
        const unsigned kvp = (unsigned)WS_KVP + ((2 * hp) * 16 + rb) * 1680;
#pragma unroll
        for (int i = 0; i < 4; ++i) {
            int at, bt;
            if (wr == 0) { at = (i & 1) ? 3 : 2; bt = i >> 1; }
            else { at = (i == 0 || i == 2) ? 4 : (i == 1 ? 3 : 2); bt = (i == 0) ? 1 : 2; }
            const int d = 16 * bt + l15;
            const int e0 = (at == 2 ? -8 : (at == 3 ? 8 : 24)) + 4 * g;
            const bool dv = d < 40;
            const int hd = d >= 20, he = e0 >= 20;
            if (dv && e0 >= 0 && hd == he) st16w(rws, kvp + (hd * (16 * 420) + (d - 20 * hd) * 20 + (e0 - 20 * hd)) * 4, __builtin_bit_cast(u32x4, R[i]));
            if (at == 2 && g == 0 && dv) { const float zv = R[i][0]; st4w(rws, kvp + (hd * (16 * 420) + 400 + d - 20 * hd) * 4, __float_as_uint(zv)); }
        }
    }
}

constexpr int KG_TAB = 0;
constexpr int KG_KC = 53 * 1808;
constexpr int KG_ST = KG_KC + 7 * 1312 + 48;
constexpr int KG_UT = KG_ST + 4096;
constexpr int KG_GW = KG_UT + 32 * 1280;
constexpr int K1_LDS_ALL = (KG_GW + 2048 > K1_LDS) ? KG_GW + 2048 : K1_LDS;
static_assert(K1_LDS_ALL <= 163840 && KG_ST % 16 == 0 && KL_KC % 16 == 0, "lds");

DI void k1_kgen(const u16* __restrict__ u, const KgIn& kin, const u16* __restrict__ gw, char* __restrict__ ws, LAS char* lds,
                int kb, int wave, int lane) {
    const rsrc_t rws = ws_rsrc(ws);
    const int ch = 7 * kb + (wave < 7 ? wave : 0);
    KgP PA[3];
#pragma unroll
    for (int p = 0; p < 3; ++p) PA[p] = kg_load(kin, ch, p, lane);
    const u16 ldt = kin.logdt[ch];
    if (wave < 7) {
        const float dt = kg_dt(ldt);
        f32x16 acc;
#pragma unroll
        for (int i = 0; i < 16; ++i) acc[i] = 0.f;
        KgP P0 = PA[0], P1 = PA[1], P2 = PA[2];
#pragma unroll 1
        for (int pass = 0; pass < 3; ++pass) {
            KgS S = kg_setup(P0, dt);
            KG_ASM(KG_CHAINS_1808, S, KG_TAB + wave * 256, 1808);
            kg_products<1808>(lds, KG_TAB + wave * 256, lane, acc);
            P0 = P1; P1 = P2;
        }
        kg_finish(acc, ch, rws, lds + KG_KC + wave * 1312, lane);
    } else {
        rsrc_t ru = __builtin_amdgcn_make_buffer_rsrc((void*)u, 0, L * D * 2, 0x00020000);
        rsrc_t rg = __builtin_amdgcn_make_buffer_rsrc((void*)gw, 0, D * 2, 0x00020000);
        __builtin_amdgcn_raw_ptr_buffer_load_lds(rg, (LAS void*)(lds + KG_GW), 16, lane * 16, 0, 0, 0);
        __builtin_amdgcn_raw_ptr_buffer_load_lds(rg, (LAS void*)(lds + KG_GW + 1024), 16, lane * 16, 1024, 0, 0);
#pragma unroll 4
        for (int i = 0; i < 40; ++i) {
            const int id = i * 64 + lane;
            const int row = (id * 52429) >> 22, c = id - 80 * row;
            const int f = (row & 3) | ((row >> 1) & 4);
            __builtin_amdgcn_raw_ptr_buffer_load_lds(ru, (LAS void*)(lds + KG_UT + i * 1024), 16, row * (D * 2) + (c ^ (f << 1)) * 16, 32 * kb * (D * 2), 0, 0);
        }
        WAIT_VM(0);
        const int l = lane & 15, g = lane >> 4;
        u32x4 gv[5];
#pragma unroll
        for (int i = 0; i < 5; ++i) gv[i] = *(const LAS u32x4*)(lds + KG_GW + (l + 16 * i) * 16);
#pragma unroll 2
        for (int it = 0; it < 8; ++it) {
            const int row = 4 * it + g;
            const int f = (row & 3) | ((row >> 1) & 4);
            float s0 = 0.f, s1 = 0.f;
#pragma unroll
            for (int i = 0; i < 5; ++i) {
                const u32x4 v = *(const LAS u32x4*)(lds + KG_UT + row * 1280 + ((l + 16 * i) ^ (f << 1)) * 16);
#pragma unroll
                for (int j = 0; j < 4; ++j) {
                    const unsigned a = v[j], b = gv[i][j];
                    if (j & 1) s1 = __builtin_amdgcn_fdot2_f32_bf16(__builtin_bit_cast(bf16x2, a), __builtin_bit_cast(bf16x2, b), s1, false);
                    else s0 = __builtin_amdgcn_fdot2_f32_bf16(__builtin_bit_cast(bf16x2, a), __builtin_bit_cast(bf16x2, b), s0, false);
                }
            }
            float s = s0 + s1;
            s += __builtin_bit_cast(float, __builtin_amdgcn_mov_dpp(__builtin_bit_cast(int, s), 0xB1, 0xF, 0xF, true));
            s += __builtin_bit_cast(float, __builtin_amdgcn_mov_dpp(__builtin_bit_cast(int, s), 0x4E, 0xF, 0xF, true));
            s += __builtin_bit_cast(float, __builtin_amdgcn_mov_dpp(__builtin_bit_cast(int, s), 0x141, 0xF, 0xF, true));
            s += __builtin_bit_cast(float, __builtin_amdgcn_mov_dpp(__builtin_bit_cast(int, s), 0x140, 0xF, 0xF, true));
            if (l == 0) st4w(rws, (unsigned)WS_G + (32 * kb + row) * 4, __float_as_uint(rbf(__builtin_amdgcn_rcpf(1.f + __expf(-rbf(s))))));
        }
        const int q = l >> 2, p = lane & 3;
        const int r0 = 8 * g + q;
        const int f0 = (r0 & 3) | ((r0 >> 1) & 4);
        const LAS char* p0 = lds + KG_UT + r0 * 1280 + (p & 1) * 8;
        LAS char* sw = lds + KG_ST + l * 64 + ((g ^ (l >> 1)) & 3) * 16;
        const LAS char* sr = lds + KG_ST + (lane >> 2) * 64 + (((lane & 3) ^ (lane >> 3)) & 3) * 16;
        const unsigned dst = (unsigned)WS_CH + (lane >> 2) * CHREC + 32 * kb * 2 + (lane & 3) * 16;
#pragma unroll 1
        for (int i0 = 0; i0 < 40; i0 += 4) {
            bf16x8 fr[4];
#pragma unroll
            for (int k = 0; k < 4; ++k) {
                const int c = ((2 * (i0 + k) + (p >> 1)) ^ (f0 << 1)) * 16;
                fr[k] = lds_frag_tr(p0 + c, p0 + c + 4 * 1280);
            }
#pragma unroll
            for (int k = 0; k < 4; ++k) *(LAS bf16x8*)(sw + k * 1024) = fr[k];
            WAIT_LGKM0();
            u32x4 o[4];
#pragma unroll
            for (int k = 0; k < 4; ++k) o[k] = *(const LAS u32x4*)(sr + k * 1024);
#pragma unroll
            for (int k = 0; k < 4; ++k) st16w(rws, dst + (i0 + k) * (16 * CHREC), o[k]);
            WAIT_LGKM0();
        }
    }
}


#define CODE_PTR(dst, label) do { unsigned lo_, hi_; \
    asm volatile("s_getpc_b64 s[40:41]\n" label "_ref_%=:\n\ts_add_u32 s40, s40, " label "-" label "_ref_%=\n\ts_addc_u32 s41, s41, 0\n\ts_mov_b32 %0, s40\n\ts_mov_b32 %1, s41" \
                 : "=s"(lo_), "=s"(hi_) :: "s40", "s41", "scc"); dst = (const char*)(((unsigned long long)hi_ << 32) | lo_); } while (0)
DI void code_prefetch(const char* begin, const char* end, int lane, int part, int nparts) {
    unsigned v[4];
#pragma unroll
    for (int k = 0; k < 4; ++k) {
        const char* p = begin + (part + k * nparts) * 8192 + lane * 128;
        v[k] = *(const __attribute__((address_space(1))) unsigned*)(p < end ? p : begin);
    }
    asm volatile("" :: "v"(v[0]), "v"(v[1]), "v"(v[2]), "v"(v[3]));
}

__global__ __launch_bounds__(512, 2) void k1_kernel(
    const u16* __restrict__ u, const u16* __restrict__ Are, const u16* __restrict__ Aim, const u16* __restrict__ Bre,
    const u16* __restrict__ Bim, const u16* __restrict__ Cre, const u16* __restrict__ Cim, const u16* __restrict__ logdt,
    const u16* __restrict__ wqkv, const u16* __restrict__ gw, char* __restrict__ ws) {
    extern __shared__ __attribute__((aligned(16))) char smem[];
    LAS char* lds = (LAS char*)smem;
    const int wave = __builtin_amdgcn_readfirstlane(threadIdx.x >> 6), lane = threadIdx.x & 63;
    const int xcd = blockIdx.x & 7, j = blockIdx.x >> 3;
    const int xr = xcd & 3, xc = xcd >> 2;
    if (wave == 3 && j < 4) {
        const char *b0, *b1;
        CODE_PTR(b0, "Lpf_k1_begin"); CODE_PTR(b1, "Lpf_k2_end");
        code_prefetch(b0, b1, lane, j, 4);
    }
    asm volatile("Lpf_k1_begin:");
    const int ra = (0x8530 >> (4 * (j & 3))) & 15;
    const int rc = (0x875320 >> (4 * (j >> 2))) & 15;
    const int rot = (ra + rc) % 10;
    if (blockIdx.x == 255) {
        const rsrc_t rws = ws_rsrc(ws);
        st16w(rws, (unsigned)WS_CNT + threadIdx.x * 16, (u32x4){0, 0, 0, 0});
        if (threadIdx.x < 256) st16w(rws, (unsigned)WS_CNT + 8192 + threadIdx.x * 16, (u32x4){0, 0, 0, 0});
    }
    const KgIn kin = {Are, Aim, Bre, Bim, Cre, Cim, logdt};
    if (j < 16) k1_gemm<false>(u, wqkv, kin, ws, lds, 4 * xr + (j & 3), 4 * xc + (j >> 2), rot, j * 8 + xcd, wave, lane);
    else if (j < 24) k1_gemm<true>(u, wqkv, kin, ws, lds, 4 * xr + (j & 3), 2 * xc + ((j - 16) >> 2), rot, j * 8 + xcd, wave, lane);
    else k1_kgen(u, kin, gw, ws, lds, (j - 24) * 8 + xcd, wave, lane);
}

constexpr int F_M = 0;
constexpr int F_WO = 27648;
constexpr int F_KVB = 32768;
constexpr int F_CZ = 77824;
constexpr int F_CX = 102400;
constexpr int F_CY = F_CX + 21248;
constexpr int F_RI = F_CY + 21248;
constexpr int K2_LDS = F_RI + 10240;
static_assert(K2_LDS <= 163840, "lds");
constexpr int CV_R = 4608;

typedef __attribute__((address_space(1))) unsigned gu32;
DI void arrive(char* ws, int off, int lane) {
    if (lane == 0) __hip_atomic_store((gu32*)(ws + WS_CNT + off), 1u, __ATOMIC_RELAXED, __HIP_MEMORY_SCOPE_AGENT);
}
template <int LANES>
DI void wait_flags(rsrc_t rws, int off, int lane) {
    for (int spin = 0; spin < (1 << 16); ++spin) {
        asm volatile("" ::: "memory");
        const u32x4 v = __builtin_amdgcn_raw_buffer_load_b128(rws, (lane & (LANES - 1)) * 16, (int)WS_CNT + off, 16);
        if (__all((v[0] & v[1] & v[2] & v[3]) == 1u)) break;
        __builtin_amdgcn_s_sleep(2);
    }
    asm volatile("" ::: "memory");
}

typedef unsigned u32x4_a4 __attribute__((ext_vector_type(4), aligned(4)));
typedef unsigned u32x2_a4 __attribute__((ext_vector_type(2), aligned(4)));
struct KrT { u32x4 a; u32x2 b; unsigned k0; };
DI KrT kr_load(const char* tab, int t) {
    const int o = t < 128 ? (t >> 1) * 4 : (t < 192 ? 256 + 4 * (t - 128) : (t < 224 ? 512 + 8 * (t - 192) : 768 + 16 * (t - 224)));
    KrT r;
    r.a = *(const u32x4_a4*)(tab + o);
    r.b = *(const u32x2_a4*)(tab + o + 16);
    r.k0 = *(const unsigned*)(tab + 640 * 2);
    return r;
}
template <int COPIES>
DI void kr_put(LAS char* dst, unsigned d0, unsigned d1, unsigned d2, unsigned d3, unsigned d4,
               unsigned s0, unsigned s1, unsigned s2, unsigned s3, unsigned s4) {
    if (COPIES & 1) *(LAS u32x4*)(dst) = (u32x4){d0, d1, d2, d3};
    if (COPIES & 2) *(LAS u32x4*)(dst + 4160) = (u32x4){s0, s1, s2, s3};
    if (COPIES & 4) *(LAS u32x4*)(dst + 2 * 4160) = (u32x4){d1, d2, d3, d4};
    if (COPIES & 8) *(LAS u32x4*)(dst + 3 * 4160) = (u32x4){s1, s2, s3, s4};
}
#define PERM(hi, lo, sel) __builtin_amdgcn_perm(hi, lo, sel)
template <int COPIES>
DI void kr_build(LAS char* cv, const KrT& T, int t) {
    LAS char* dst = cv + CV_R + t * 16;
    const unsigned a0 = T.a[0], a1 = T.a[1], a2 = T.a[2], a3 = T.a[3], b0 = T.b[0], b1 = T.b[1];
    if (t < 128) {
        const unsigned ab = __builtin_amdgcn_alignbyte(a1, a0, (t & 1) * 2);
        const unsigned AA = PERM(ab, ab, 0x01000100u), BB = PERM(ab, ab, 0x03020302u);
        const unsigned TB = (t & 1) ? BB : ab, AT = (t & 1) ? ab : AA;
        kr_put<COPIES>(dst, AA, AA, TB, BB, BB, AA, AT, BB, BB, BB);
    } else if (t < 192) {
        const unsigned AA = PERM(a0, a0, 0x01000100u), BB = PERM(a0, a0, 0x03020302u), CC = PERM(a1, a1, 0x01000100u), BC = PERM(a1, a0, 0x05040302u);
        kr_put<COPIES>(dst, AA, a0, BB, CC, CC, AA, BB, BC, CC, t == 191 ? a1 : CC);
    } else if (t < 224) {
        const unsigned T44 = PERM(a2, a2, 0x01000100u), T22 = PERM(a1, a1, 0x01000100u);
        const bool last = t == 223;
        kr_put<COPIES>(dst, PERM(a0, a0, 0x01000100u), PERM(a1, a0, 0x05040302u), T22, PERM(a2, a1, 0x05040302u), last ? a2 : T44,
               a0, T22, a1, T44, last ? PERM(a3, a2, 0x05040302u) : a2);
    } else {
        kr_put<COPIES>(dst, a0, a1, a2, a3, b0, __builtin_amdgcn_alignbyte(a1, a0, 2), __builtin_amdgcn_alignbyte(a2, a1, 2),
               __builtin_amdgcn_alignbyte(a3, a2, 2), __builtin_amdgcn_alignbyte(b0, a3, 2), __builtin_amdgcn_alignbyte(b1, b0, 2));
    }
    if ((COPIES & 1) && t < 8)
        *(LAS u32x4*)(cv + CV_R + (t >> 1) * 4160 + (256 + (t & 1)) * 16) = (u32x4){t == 0 ? T.k0 : 0u, 0u, 0u, 0u};
}
#define SB0() __builtin_amdgcn_sched_barrier(0)
template <int CLO, int CHI, class H>
DI void conv_2rho(const LAS char* cv, int rho0, int lane, f32x4 (&acc)[8], H hook) {
    const int r = lane & 15, g = lane >> 4, p = (-r) & 3;
    const LAS char* pa = cv + CV_R + p * 4160 + 2 * (2032 - r - p + 8 * g) - 512 * 7 - 64 * rho0;
    const LAS char* pb = cv + 2 * (240 + 16 * r + 8 * g) - 64 * rho0;
    const LAS char* ph = pa + 8;
    asm volatile("" : "+v"(ph));
    bf16x8 fa[8], fb[8];
    auto rda = [&](int a, int d) {
        const u32x2 lo = *(const LAS u32x2*)(pa + 512 * (7 - a) - 64 * d);
        const u32x2 hi = *(const LAS u32x2*)(ph + 512 * (7 - a) - 64 * d);
        fa[a] = asfrag((u32x4){lo[0], lo[1], hi[0], hi[1]});
    };
    auto rdb = [&](int b, int d) { fb[b] = *(const LAS bf16x8*)(pb + 512 * b - 64 * d); };
    rda(0, 0);
#pragma unroll
    for (int b = CHI; b >= 0; --b) rdb(b, 0);
#pragma unroll
    for (int a = 1; a <= CHI; ++a) { asm volatile("" ::: "memory"); rda(a, 0); }
    SB0();
#pragma unroll
    for (int d = 0; d < 2; ++d)
#pragma unroll
        for (int a = 0; a <= CHI; ++a) {
#pragma unroll
            for (int b = CHI - a; b >= 0; --b)
                if (a + b >= CLO) acc[a + b] = mfma16(fa[a], fb[b], acc[a + b]);
            SB0();
            if (d == 0) { rdb(CHI - a, 1); rda(a, 1); }
            hook(8 * d + a);
            SB0();
        }
}
DI u32x2 conv_u(const LAS char* cv, int c, int lane) { return *(const LAS u32x2*)(cv + 512 + (16 * (16 * c + (lane & 15)) + 4 * (lane >> 4)) * 2); }
DI void conv_out(rsrc_t rws, u32x2 uu, int ch, int c, float ds, f32x4 o, int lane) {
    const int t = 16 * (16 * c + (lane & 15)) + 4 * (lane >> 4);
    const unsigned u0 = uu[0], u1 = uu[1];
    st8w(rws, (unsigned)WS_AT + (ch * L + t) * 2,
         (u32x2){pk2(__builtin_fmaf(bflo(u0), ds, o[0]), __builtin_fmaf(bfhi(u0), ds, o[1])),
                 pk2(__builtin_fmaf(bflo(u1), ds, o[2]), __builtin_fmaf(bfhi(u1), ds, o[3]))});
}

__global__ __launch_bounds__(512, 2) void k2_kernel(const u16* __restrict__ dskip, const u16* __restrict__ wo, char* __restrict__ ws,
                                                    u16* __restrict__ out) {
    extern __shared__ __attribute__((aligned(16))) char smem[];
    LAS char* lds = (LAS char*)smem;
    const int wave = __builtin_amdgcn_readfirstlane(threadIdx.x >> 6), lane = threadIdx.x & 63;
    const int tid = wave * 64 + lane, wl = wave & 3, grp = wave >> 2, tg = tid & 255;
    const int l15 = lane & 15, g = lane >> 4;
    const int bid = blockIdx.x, xcd = bid & 7, j = bid >> 3;
    const int rb = 8 * (xcd & 3) + (j & 7), cg = 4 * (xcd >> 2) + (j >> 3);
    const int hM = 4 * xcd + (j >> 3), cgM = j & 7;
    const int P = bid >> 1, half = bid & 1;
    const int chG = 5 * P + 3 * half + grp, chZ = 5 * P + 2;
    const rsrc_t rws = ws_rsrc(ws);
    LAS char* cv = lds + (grp ? F_CY : F_CX);
    const rsrc_t rout = __builtin_amdgcn_make_buffer_rsrc((void*)out, 0, L * D * 2, 0x00020000);
    const int recG = (int)WS_CH + chG * CHREC, recZ = (int)WS_CH + chZ * CHREC;
    const KrT tG = kr_load(ws + recG + 4096, tg);
    const KrT tZ = kr_load(ws + recZ + 4096, tg);
    {
        __builtin_amdgcn_raw_ptr_buffer_load_lds(rws, (LAS void*)(cv + 512 + wl * 1024), 16, lane * 16, recG + wl * 1024, 0, 0);
        if (grp == 0) __builtin_amdgcn_raw_ptr_buffer_load_lds(rws, (LAS void*)(lds + F_CZ + 512 + wl * 1024), 16, lane * 16, recZ + wl * 1024, 0, 0);
        rsrc_t rk = __builtin_amdgcn_make_buffer_rsrc((void*)(ws + WS_KVP + (size_t)hM * 26880), 0, 26880, 0x00020000);
#pragma unroll
        for (int k = 0; k < 3; ++k)
            __builtin_amdgcn_raw_ptr_buffer_load_lds(rk, (LAS void*)(lds + F_M + (wave + 8 * k) * 1024), 16, lane * 16, (wave + 8 * k) * 1024, 0, 0);
        if (wave < 3) __builtin_amdgcn_raw_ptr_buffer_load_lds(rk, (LAS void*)(lds + F_M + (wave + 24) * 1024), 16, lane * 16, (wave + 24) * 1024, 0, 0);
        else {
            rsrc_t rw = __builtin_amdgcn_make_buffer_rsrc((void*)wo, 0, D * D * 2, 0x00020000);
            const int id = (wave - 3) * 64 + lane, e = (id * 205) >> 11, c = id - 10 * e;
            __builtin_amdgcn_raw_ptr_buffer_load_lds(rw, (LAS void*)(lds + F_WO + (wave - 3) * 1024), 16, e * (D * 2) + 16 * c,
                                                     20 * hM * (D * 2) + 160 * cgM, 0, 0);
        }
    }
    const float dsG = bf2f(dskip[chG]), dsZ = bf2f(dskip[chZ]);
    const int rt = 4 * rb + wl;
    const float gt = ((const float*)(ws + WS_G))[16 * rt + l15];
    if (tg < 32) *(LAS u32x4*)(cv + tg * 16) = (u32x4){0, 0, 0, 0};
    if (tid < 32) *(LAS u32x4*)(lds + F_CZ + tid * 16) = (u32x4){0, 0, 0, 0};
    if (tid < 160) *(LAS u32x4*)(lds + F_KVB + tid * 16) = (u32x4){0, 0, 0, 0};
    kr_build<15>(cv, tG, tg);
    if (grp == 0) kr_build<3>(lds + F_CZ, tZ, tg); else kr_build<12>(lds + F_CZ, tZ, tg);
    WAIT_VM(0); WAIT_LGKM0(); BAR();
    if (tid < 420) {
        const int col = tid >> 2;
        const LAS char* p = lds + F_M + (tid & 3) * (4 * 1680) + col * 16;
        f32x4 sum = (*(const LAS f32x4*)(p) + *(const LAS f32x4*)(p + 1680)) + (*(const LAS f32x4*)(p + 2 * 1680) + *(const LAS f32x4*)(p + 3 * 1680));
#pragma unroll
        for (int e = 0; e < 4; ++e) {
            float v = sum[e];
            v += __builtin_bit_cast(float, __builtin_amdgcn_mov_dpp(__builtin_bit_cast(int, v), 0xB1, 0xF, 0xF, true));
            v += __builtin_bit_cast(float, __builtin_amdgcn_mov_dpp(__builtin_bit_cast(int, v), 0x4E, 0xF, 0xF, true));
            sum[e] = v;
        }
        if ((tid & 3) == 0) {
            const u32x2 v = {pk2(sum[0], sum[1]), pk2(sum[2], sum[3])};
            if (col < 100) { const int d = (col * 205) >> 10; *(LAS u32x2*)(lds + F_KVB + d * 80 + (col - 5 * d) * 8) = v; }
            else st8w(rws, (unsigned)WS_ZB + cgM * 1280 + (20 * hM + 4 * (col - 100)) * 2, v);
        }
    }
    WAIT_LGKM0(); BAR();
    u32x4 f[10];
    const char* fsrc = ws + WS_PQF + ((size_t)(rt * 20 + 10 * grp) * 64 + lane) * 16;
    auto fetch_f = [&](int i0, int i1) {
        asm volatile("" ::: "memory");
#pragma unroll
        for (int i = 0; i < 10; ++i) if (i >= i0 && i < i1) f[i] = *(const u32x4*)(fsrc + i * 1024);
        asm volatile("" ::: "memory");
    };
    f32x4 acc[8], az[8];
#pragma unroll
    for (int c = 0; c < 8; ++c) { acc[c] = (f32x4){0.f, 0.f, 0.f, 0.f}; az[c] = (f32x4){0.f, 0.f, 0.f, 0.f}; }
    const int nt = half ? 2 : 6, cz0 = half ? 6 : 0;
    if (grp == 0) {
        conv_2rho<0, 7>(lds + F_CX, 2 * wl, lane, acc, [&](int st) {
            if (st == 0) fetch_f(0, 4);
            if (st == 3) fetch_f(4, 7);
            if (st == 8) fetch_f(7, 10); });
        auto hz = [&](int) {};
        if (half == 0) conv_2rho<0, 5>(lds + F_CZ, 2 * wl, lane, az, hz);
        else conv_2rho<6, 7>(lds + F_CZ, 2 * wl, lane, az, hz);
    } else {
        const int q = l15 >> 2, p = lane & 3;
        bf16x8 fb[3], fa[2];
#pragma unroll
        for (int it = 0; it < 3; ++it) {
            const int tI = wl + 4 * it < 10 ? wl + 4 * it : wl, ct = tI >= 5 ? tI - 5 : tI;
            const LAS char* p0 = lds + F_WO + (8 * g + q) * 160 + ct * 32 + p * 8;
            fb[it] = lds_frag_tr(p0, p0 + 4 * 160);
        }
#pragma unroll
        for (int dt = 0; dt < 2; ++dt) fa[dt] = *(const LAS bf16x8*)(lds + F_KVB + (16 * dt + l15) * 80 + g * 16);
#pragma unroll
        for (int it = 0; it < 3; ++it) {
            const int tI = wl + 4 * it;
            if (tI < 10) {
                const int dt = tI >= 5, ct = tI - 5 * dt;
                const f32x4 dd = mfma16(dt ? fa[1] : fa[0], fb[it], (f32x4){0.f, 0.f, 0.f, 0.f});
                const int gl = 4 * dt + g;
                if (gl < 5) {
                    const int G = 5 * hM + gl, s = G >> 3, hf = (G >> 2) & 1, gg = G & 3;
                    st8w(rws, (unsigned)WS_MF + (((cgM * 5 + ct) * 20 + s) * 64 + l15 + 16 * gg) * 16 + hf * 8,
                         (u32x2){pk2(dd[0], dd[1]), pk2(dd[2], dd[3])});
                }
            }
        }
        conv_2rho<0, 7>(lds + F_CY, 2 * wl, lane, acc, [&](int st) {
            if (st == 1) fetch_f(0, 4);
            if (st == 4) fetch_f(4, 7);
            if (st == 9) fetch_f(7, 10);
            if (st == 12) {
                WAIT_VM(10);
                arrive(ws, cgM * 512 + (hM * 4 + wl) * 4, lane);
            } });
    }
    WAIT_LGKM0(); BAR();
    u32x4 mfl = {0, 0, 0, 0};
    {
        LAS f32x4* red = (LAS f32x4*)(lds + F_M + grp * 24576) + lane;
        LAS f32x4* pz = (LAS f32x4*)(lds + F_M + 2 * 24576) + lane;
        f32x4 o0, o1;
#pragma unroll
        for (int c = 0; c < 8; ++c) {
            if ((c >> 1) != wl) red[(wl * 6 + c - (c > 2 * wl + 1 ? 2 : 0)) * 64] = acc[c];
            else if (c & 1) o1 = acc[c];
            else o0 = acc[c];
        }
        if (grp == 0) {
            if (half == 0) {
#pragma unroll
                for (int c = 0; c < 6; ++c) pz[(wl * 6 + c) * 64] = az[c];
            } else {
#pragma unroll
                for (int c = 0; c < 2; ++c) pz[(wl * 6 + c) * 64] = az[6 + c];
            }
        }
        const u32x2 u0 = conv_u(cv, 2 * wl, lane), u1 = conv_u(cv, 2 * wl + 1, lane);
        u32x2 uz0, uz1;
        if (grp) { uz0 = conv_u(lds + F_CZ, cz0 + (wl < nt ? wl : 0), lane); uz1 = conv_u(lds + F_CZ, cz0 + (wl + 4 < nt ? wl + 4 : 0), lane); }
        WAIT_LGKM0(); BAR();
        if (wave == 0) mfl = __builtin_amdgcn_raw_buffer_load_b128(rws, (lane & 31) * 16, (int)WS_CNT + cg * 512, 16);
        asm volatile("" ::: "memory");
#pragma unroll
        for (int sw = 0; sw < 4; ++sw) {
            if (sw != wl) {
                const int k = 2 * wl - (wl > sw ? 2 : 0);
                o0 += red[(sw * 6 + k) * 64];
                o1 += red[(sw * 6 + k + 1) * 64];
            }
        }
        conv_out(rws, u0, chG, 2 * wl, dsG, o0, lane);
        conv_out(rws, u1, chG, 2 * wl + 1, dsG, o1, lane);
        if (grp) {
            if (wl < nt) conv_out(rws, uz0, chZ, cz0 + wl, dsZ, (pz[wl * 64] + pz[(6 + wl) * 64]) + (pz[(12 + wl) * 64] + pz[(18 + wl) * 64]), lane);
            if (wl + 4 < nt) conv_out(rws, uz1, chZ, cz0 + wl + 4, dsZ, (pz[(wl + 4) * 64] + pz[(10 + wl) * 64]) + (pz[(16 + wl) * 64] + pz[(22 + wl) * 64]), lane);
        }
    }
    if (wave == 0 && !__all((mfl[0] & mfl[1] & mfl[2] & mfl[3]) == 1u)) wait_flags<32>(rws, cg * 512, lane);
    WAIT_LGKM0(); BAR();
    int mcur = (wave + 8 * (j & 7)) * 1024;
    const int msrc = (int)WS_MF + cg * 102400;
    auto mslab = [&]() {
        __builtin_amdgcn_raw_ptr_buffer_load_lds(rws, (LAS void*)(lds + F_M + mcur), 16, lane * 16, msrc + mcur, 0, 16);
        mcur += 8192;
        mcur = mcur >= 102400 ? wave * 1024 : mcur;
    };
    if (lane < 40) __builtin_amdgcn_raw_ptr_buffer_load_lds(rws, (LAS void*)(lds + F_RI + wave * 1280), 16, lane * 16, (int)WS_ZB + cg * 1280 + grp * 640, 0, 16);
    asm volatile("" ::: "memory");
#pragma unroll
    for (int k = 0; k < 13; ++k) mslab();
    WAIT_VM(13);
    arrive(ws, 4096 + (P >> 4) * 1024 + ((bid & 31) * 8 + wave) * 4, lane);
    auto normalize = [&]() {
        const LAS char* zb = lds + F_RI + wave * 1280 + 8 * g;
        u32x2 z0[10], z1[10];
#pragma unroll
        for (int i = 0; i < 10; ++i) { z0[i] = *(const LAS u32x2*)(zb + 64 * i); asm volatile("" ::: "memory"); z1[i] = *(const LAS u32x2*)(zb + 64 * i + 32); asm volatile("" ::: "memory"); }
        const int base = g - 5 * l15;
        f32x4 den0 = {0.f, 0.f, 0.f, 0.f}, den1 = {0.f, 0.f, 0.f, 0.f};
#pragma unroll
        for (int i = 0; i < 10; ++i) {
            const unsigned m0 = (unsigned)(base + 8 * i) < 5u ? ~0u : 0u, m1 = (unsigned)(base + 8 * i + 4) < 5u ? ~0u : 0u;
            const bf16x8 za = asfrag((u32x4){z0[i][0] & m0, z0[i][1] & m0, z1[i][0] & m1, z1[i][1] & m1});
            if (i & 1) den1 = mfma16(za, asfrag(f[i]), den1); else den0 = mfma16(za, asfrag(f[i]), den0);
        }
        const f32x4 den = den0 + den1;
        f32x4 rv;
#pragma unroll
        for (int r = 0; r < 4; ++r) rv[r] = __builtin_amdgcn_rcpf(den[r] + 1e-6f);
        LAS float* ri = (LAS float*)(lds + F_RI + wave * 1280) + l15 * 20;
        WAIT_LGKM0();
        *(LAS f32x4*)(ri + 4 * g) = rv;
        WAIT_LGKM0();
        float r0[10], r1[10];
#pragma unroll
        for (int i = 0; i < 5; ++i) {
            const LAS float* pa = ri + (((8 * i + g) * 205) >> 10);
            const LAS float* pb = ri + (((8 * i + 4 + g) * 205) >> 10);
            r0[i] = pa[0]; r0[i + 5] = pa[8]; r1[i] = pb[0]; r1[i + 5] = pb[8];
        }
#pragma unroll
        for (int i = 0; i < 10; ++i) {
#pragma unroll
            for (int k = 0; k < 4; ++k) {
                const unsigned v = f[i][k];
                const f32x2 x = {bflo(v), bfhi(v)};
                f[i][k] = pk2f(x * (k < 2 ? r0[i] : r1[i]));
            }
        }
    };
    normalize();
    WAIT_VM(1); WAIT_LGKM0(); BAR();
    f32x4 ac[5];
#pragma unroll
    for (int c = 0; c < 5; ++c) ac[c] = (f32x4){0.f, 0.f, 0.f, 0.f};
    const LAS char* pm = lds + F_M + grp * 10240 + lane * 16;
    {
        bf16x8 fm[2][5];
#pragma unroll
        for (int c = 0; c < 5; ++c) fm[0][c] = *(const LAS bf16x8*)(pm + (c * 20) * 1024);
#pragma unroll
        for (int i = 0; i < 10; ++i) {
            if (i < 9) {
#pragma unroll
                for (int c = 0; c < 5; ++c) fm[(i + 1) & 1][c] = *(const LAS bf16x8*)(pm + (c * 20 + i + 1) * 1024);
            }
            SB0();
#pragma unroll
            for (int c = 0; c < 5; ++c) ac[c] = mfma16(fm[i & 1][c], asfrag(f[i]), ac[c]);
            SB0();
            if (i == 4 && wave == 4) {
                wait_flags<64>(rws, 4096 + cg * 1024, lane);
#pragma unroll
                for (int k = 0; k < 10; ++k)
                    __builtin_amdgcn_raw_ptr_buffer_load_lds(rws, (LAS void*)(lds + F_CX + k * 1024), 16,
                                                             ((lane >> 3) * L) * 2 + (lane & 7) * 16, (int)WS_AT + ((80 * cg + 8 * k) * L + 64 * rb) * 2, 0, 16);
            }
        }
    }
    LAS f32x4* ex = (LAS f32x4*)(lds + F_CY) + lane;
    if (grp == 0) { ex[(wl * 5 + 3) * 64] = ac[3]; ex[(wl * 5 + 4) * 64] = ac[4]; asm volatile("" ::: "memory"); }
    else { ex[(wl * 5 + 0) * 64] = ac[0]; ex[(wl * 5 + 1) * 64] = ac[1]; ex[(wl * 5 + 2) * 64] = ac[2]; }
    if (wave == 4) WAIT_VM(0);
    WAIT_LGKM0(); BAR();
    const float og = 1.f - gt;
    const int q = l15 >> 2, p = lane & 3;
    auto finish = [&](int c, f32x4 v) {
        v += ex[(wl * 5 + c) * 64];
        const s16x4 av = __builtin_amdgcn_ds_read_tr16_b64_v4i16((LAS s16x4*)(lds + F_CX + (16 * c + 4 * g + q) * 128 + (16 * wl + 4 * p) * 2));
        const u32x2 aa = __builtin_bit_cast(u32x2, av);
        const unsigned a0 = aa[0], a1 = aa[1];
        st8w(rout, ((16 * rt + l15) * D + 80 * cg + 16 * c + 4 * g) * 2,
            (u32x2){pk2(__builtin_fmaf(gt, bflo(a0), og * v[0]), __builtin_fmaf(gt, bfhi(a0), og * v[1])),
                    pk2(__builtin_fmaf(gt, bflo(a1), og * v[2]), __builtin_fmaf(gt, bfhi(a1), og * v[3]))});
    };
    if (grp == 0) { finish(0, ac[0]); finish(1, ac[1]); finish(2, ac[2]); }
    else { finish(3, ac[3]); finish(4, ac[4]); }
    asm volatile("Lpf_k2_end:");
}

extern "C" void kernel_launch(void* const* d_in, const int* in_sizes, int n_in, void* d_out, int out_size, void* d_ws,
                              size_t ws_size, hipStream_t stream) {
    const u16* u = (const u16*)d_in[0];
    const u16* Are = (const u16*)d_in[1];
    const u16* Aim = (const u16*)d_in[2];
    const u16* Bre = (const u16*)d_in[3];
    const u16* Bim = (const u16*)d_in[4];
    const u16* Cre = (const u16*)d_in[5];
    const u16* Cim = (const u16*)d_in[6];
    const u16* logdt = (const u16*)d_in[7];
    const u16* dskip = (const u16*)d_in[8];
    const u16* wqkv = (const u16*)d_in[10];
    const u16* wo = (const u16*)d_in[11];
    const u16* gw = (const u16*)d_in[12];
    char* ws = (char*)d_ws;
    (void)hipFuncSetAttribute((const void*)k1_kernel, hipFuncAttributeMaxDynamicSharedMemorySize, K1_LDS_ALL);
    (void)hipFuncSetAttribute((const void*)k2_kernel, hipFuncAttributeMaxDynamicSharedMemorySize, K2_LDS);
    k1_kernel<<<256, 512, K1_LDS_ALL, stream>>>(u, Are, Aim, Bre, Bim, Cre, Cim, logdt, wqkv, gw, ws);
    k2_kernel<<<256, 512, K2_LDS, stream>>>(dskip, wo, ws, (u16*)d_out);
}
```
